```python
import torch

def _softmax(x, axis=-1):
    return torch.softmax(x, dim=axis)
def _gelu(x):
    return torch.nn.functional.gelu(x, approximate="tanh")
def _silu(x):
    return torch.nn.functional.silu(x)
def _sigmoid(x):
    return torch.sigmoid(x)
def _layernorm(x, axis=-1, eps=1e-5):
    m = x.mean(dim=axis, keepdim=True)
    v = x.var(dim=axis, keepdim=True, unbiased=False)
    return (x - m) / torch.sqrt(v + eps)
def _scatter_add(tgt, idx, src, axis=0):
    return tgt.clone().index_add_(axis, idx, src)
def _segment_sum(src, seg, n):
    out = torch.zeros((n,) + src.shape[1:], dtype=src.dtype)
    return out.index_add_(0, seg, src)
def _pad(x, pw):
    flat = tuple(v for pair in reversed(pw) for v in pair)
    return torch.nn.functional.pad(x, flat)

kernel_name = 'recurrent_scan__sliding_local_dilated__glu_gated_bilinear__prompt'

def setup_inputs():

    _w = (lambda a: (lambda t: t.to(torch.bfloat16) if t.is_floating_point() else t)(torch.as_tensor(a)))
    def _A_setup():
        B, S, D = 1, 2048, 448
        return {
            "x": _w((lambda *s: torch.randn(*s, dtype=torch.bfloat16) * 0.05)(B, S, D)),
            "wqkv": _w((lambda *s: torch.randn(*s, dtype=torch.bfloat16) * 0.2)(D, 3 * D)),
            "wo": _w((lambda *s: torch.randn(*s, dtype=torch.bfloat16) * 0.2)(D, D)),
        }
    _A_in = _A_setup()

    def _B_setup():
        B, S, D, F = 1, 2048, 448, 1792
        return {
            "x": _w((lambda *s: torch.randn(*s, dtype=torch.bfloat16) * 0.05)(B, S, D)),
            "w_gate": _w((lambda *s: torch.randn(*s, dtype=torch.bfloat16) * 0.1)(D, F) * (D ** -0.5)),
            "w_up": _w((lambda *s: torch.randn(*s, dtype=torch.bfloat16) * 0.1)(D, F) * (D ** -0.5)),
            "w_down": _w((lambda *s: torch.randn(*s, dtype=torch.bfloat16) * 0.1)(F, D) * (F ** -0.5)),
        }
    _B_in = _B_setup()

    return {
        "A_x": _A_in["x"],
        "A_wqkv": _A_in["wqkv"],
        "A_wo": _A_in["wo"],
        "B_x": _B_in["x"],
        "B_w_gate": _B_in["w_gate"],
        "B_w_up": _B_in["w_up"],
        "B_w_down": _B_in["w_down"],
    }

def reference(A_x, A_wqkv, A_wo, B_x, B_w_gate, B_w_up, B_w_down):
    def _A_ref(x, wqkv, wo):
        B, S, D = x.shape
        H = 16; Dh = D // H
        W, dil = 8, 2
        qkv = (lambda x, *d: x.permute(*d))((x @ wqkv).reshape(B, S, 3, H, Dh), 2, 0, 3, 1, 4)
        q, k, v = qkv[0], qkv[1], qkv[2]
        scale = Dh ** -0.5
        pos = torch.arange(S)
        off = torch.arange(W) * dil
        idx = pos[:, None] - off[None, :]
        mask = idx >= 0
        idx_c = torch.where(mask, idx, 0).reshape(-1)
        kg = k[:, :, idx_c, :].reshape(B, H, S, W, Dh)
        vg = v[:, :, idx_c, :].reshape(B, H, S, W, Dh)
        logits = torch.einsum("bhsd,bhswd->bhsw", q, kg) * scale
        logits = torch.where(mask[None, None, :, :], logits, -1e9)
        attn = _softmax(logits, axis=-1)
        o = torch.einsum("bhsw,bhswd->bhsd", attn, vg)
        return (lambda x, *d: x.permute(*d))(o, 0, 2, 1, 3).reshape(B, S, D) @ wo

    def _B_ref(x, w_gate, w_up, w_down):
        g = torch.einsum("bsd,df->bsf", x, w_gate)
        u = torch.einsum("bsd,df->bsf", x, w_up)
        h = g * u
        return torch.einsum("bsf,fd->bsd", h, w_down)

    x = A_x
    xn = _layernorm(x, axis=-1)
    s = _A_ref(xn, A_wqkv, A_wo)
    g = _silu(_B_ref(xn, B_w_gate, B_w_up, B_w_down))
    return x + s * g
```

```cpp
#include <hip/hip_runtime.h>
#include <hip/hip_bf16.h>
typedef unsigned short u16;
__device__ __forceinline__ float bf2f(u16 x){ return __uint_as_float(((unsigned)x)<<16); }
__device__ __forceinline__ u16 f2bf(float f){ __hip_bfloat16 b = __float2bfloat16(f); return *(u16*)&b; }
__device__ __forceinline__ float rbf(float f){ return bf2f(f2bf(f)); }
#define S 2048
#define D 448
#define F 1792
__global__ void k_ln(const u16* x, u16* xn){
  int row = blockIdx.x; int t = threadIdx.x;
  float v[7]; float s=0;
  for(int i=0;i<7;i++){ v[i]=bf2f(x[row*D+t+64*i]); s+=v[i]; }
  for(int o=32;o>0;o>>=1) s+=__shfl_xor(s,o);
  float mean=s/D; float q=0;
  for(int i=0;i<7;i++){ float d=v[i]-mean; q+=d*d; }
  for(int o=32;o>0;o>>=1) q+=__shfl_xor(q,o);
  float m=rbf(mean); float var=rbf(q/D);
  float den=rbf(sqrtf(rbf(var+1e-5f)));
  for(int i=0;i<7;i++){ xn[row*D+t+64*i]=f2bf(rbf(v[i]-m)/den); }
}
__global__ void k_gemm(const u16* A,const u16* B,u16* C,int M,int N,int K){
  int n=blockIdx.x*64+threadIdx.x; int m=blockIdx.y*4+threadIdx.y;
  if(n>=N) return;
  float acc=0; for(int k=0;k<K;k++) acc+=bf2f(A[m*K+k])*bf2f(B[k*N+n]);
  C[m*N+n]=f2bf(acc);
}
__global__ void k_attn(const u16* qkv,u16* o){
  int idx=blockIdx.x*256+threadIdx.x; int s=idx/16,h=idx%16;
  float lg[8]; float mx=-1e30f;
  const float scale=rbf(0.18898223650461362f);
  for(int w=0;w<8;w++){ int p=s-2*w; float l;
    if(p>=0){ float a=0; for(int d=0;d<28;d++) a+=bf2f(qkv[s*1344+h*28+d])*bf2f(qkv[p*1344+448+h*28+d]); l=rbf(rbf(a)*0.18898223650461362f);} else l=rbf(-1e9f);
    lg[w]=l; mx=fmaxf(mx,l);}
  float sum=0; for(int w=0;w<8;w++){ lg[w]=__expf(lg[w]-mx); sum+=lg[w]; }
  for(int w=0;w<8;w++) lg[w]=rbf(lg[w]/sum);
  for(int d=0;d<28;d++){ float a=0; for(int w=0;w<8;w++){ int p=s-2*w; if(p<0)p=0; a+=lg[w]*bf2f(qkv[p*1344+896+h*28+d]);} o[s*448+h*28+d]=f2bf(a);}
}
__global__ void k_mul(const u16* g,const u16* u,u16* h,int n){ int i=blockIdx.x*256+threadIdx.x; if(i<n) h[i]=f2bf(bf2f(g[i])*bf2f(u[i])); }
__global__ void k_fin(const u16* x,const u16* s,const u16* f,u16* out,int n){ int i=blockIdx.x*256+threadIdx.x; if(i<n){ float ff=bf2f(f[i]); float g=rbf(ff/(1.f+__expf(-ff))); out[i]=f2bf(bf2f(x[i])+rbf(bf2f(s[i])*g)); } }
extern "C" void kernel_launch(void* const* d_in, const int* in_sizes, int n_in, void* d_out, int out_size, void* d_ws, size_t ws_size, hipStream_t stream){
  const u16* x=(const u16*)d_in[0]; const u16* wqkv=(const u16*)d_in[1]; const u16* wo=(const u16*)d_in[2];
  const u16* wg=(const u16*)d_in[4]; const u16* wu=(const u16*)d_in[5]; const u16* wd=(const u16*)d_in[6];
  u16* ws=(u16*)d_ws; u16* xn=ws; u16* qkv=xn+S*D; u16* o=qkv+S*1344; u16* g=o+S*D; u16* u=g+S*F; u16* h=u+S*F; u16* sb=h+S*F; u16* fb=sb+S*D;
  k_ln<<<S,64,0,stream>>>(x,xn);
  k_gemm<<<dim3(21,S/4),dim3(64,4),0,stream>>>(xn,wqkv,qkv,S,1344,D);
  k_gemm<<<dim3(28,S/4),dim3(64,4),0,stream>>>(xn,wg,g,S,F,D);
  k_gemm<<<dim3(28,S/4),dim3(64,4),0,stream>>>(xn,wu,u,S,F,D);
  k_attn<<<S*16/256,256,0,stream>>>(qkv,o);
  k_mul<<<S*F/256,256,0,stream>>>(g,u,h,S*F);
  k_gemm<<<dim3(7,S/4),dim3(64,4),0,stream>>>(o,wo,sb,S,D,D);
  k_gemm<<<dim3(7,S/4),dim3(64,4),0,stream>>>(h,wd,fb,S,D,F);
  k_fin<<<S*D/256,256,0,stream>>>(x,sb,fb,(u16*)d_out,S*D);
}
```

```cpp
#include <hip/hip_runtime.h>
#pragma clang diagnostic ignored "-Wint-to-pointer-cast"
#include <utility>

typedef unsigned short u16;
typedef unsigned int u32;
typedef unsigned long long u64;
typedef __attribute__((ext_vector_type(8))) short bf16x8;
typedef __attribute__((ext_vector_type(4))) short s16x4;
typedef __attribute__((ext_vector_type(4))) float f32x4;
typedef __attribute__((ext_vector_type(2))) float f32x2;
typedef __attribute__((ext_vector_type(4))) u32 u32x4;
typedef __attribute__((ext_vector_type(2))) u32 u32x2;
typedef __attribute__((ext_vector_type(4))) int srd_t;
#define LAS __attribute__((address_space(3)))
#define DEV __device__ __forceinline__
#define INL __attribute__((always_inline))

template <class F, int... I> DEV void sfor_impl(F&& f, std::integer_sequence<int, I...>) { (f(std::integral_constant<int, I>{}), ...); }
template <int N, class F> DEV void sfor(F&& f) { sfor_impl(f, std::make_integer_sequence<int, N>{}); }

constexpr int S_ = 2048, D_ = 448, F_ = 1792;
constexpr int XROWB = D_ * 2;
constexpr int GROWB = F_ * 2;
constexpr int QROWB = 3 * D_ * 2;
constexpr int T2ROWS = 79;
constexpr int QROWS = 171;
constexpr int WS_EPOCH = 0;
constexpr int WS_HFLAG = 4096;
constexpr int WS_OFLAG = 16384;
constexpr int WS_STAT = 32768;
constexpr int WS_ACT = 65536;
constexpr int ACT_TILE = 72 * 5120;
constexpr int WS_END = WS_ACT + 26 * ACT_TILE;
constexpr int LDS_BYTES = 163840;
constexpr int L_STAT = 160512;
constexpr int L_TAG = 162048;
constexpr int L_JUNK = 148224;
template <bool FFN> struct Cfg {
  static constexpr int NRB = FFN ? 10 : 12;
  static constexpr int NMAT = FFN ? 2 : 3;
  static constexpr int XSLOT = NRB * 2048 + 256;
  static constexpr int NWS = FFN ? 4 : 3;
  static constexpr int WSLOT = NMAT * 8192;
  static constexpr int LX = 0, LW = 3 * XSLOT;
  static constexpr int XP = NRB / 2;
  static constexpr int WP = NMAT * 2;
  static constexpr int ONES_RB = FFN ? 4 : 11;
};
constexpr int L_EB = 127744;
constexpr int L_WO = 103168;
constexpr int RSLOT = 20480;
constexpr int L_FS = 81920;
constexpr int IMGROW = 592;

constexpr u32 SPIN_CAP = 1u << 22;

DEV srd_t make_srd(const void* p, u32 bytes) {
  u64 a = (u64)p; srd_t s; s[0] = (int)(u32)a; s[1] = (int)((u32)(a >> 32) & 0xffffu); s[2] = (int)bytes; s[3] = 0x00020000; return s;
}
template <int LI, int SI> DEV void dma(u32 voff, srd_t srd, u32 sb, u32 lb) {
  u32 t;
  asm volatile("s_add_u32 m0, %4, %5\n\ts_add_u32 %0, %3, %6\n\tbuffer_load_dwordx4 %1, %2, %0 offen lds"
               : "=&s"(t) : "v"(voff), "s"(srd), "s"(sb), "s"(lb), "i"(LI), "i"(SI) : "memory", "scc");
}
template <int LI, int SI> DEV void dma_sc1(u32 voff, srd_t srd, u32 sb, u32 lb) {
  u32 t;
  asm volatile("s_add_u32 m0, %4, %5\n\ts_add_u32 %0, %3, %6\n\tbuffer_load_dwordx4 %1, %2, %0 offen sc1 lds"
               : "=&s"(t) : "v"(voff), "s"(srd), "s"(sb), "s"(lb), "i"(LI), "i"(SI) : "memory", "scc");
}
DEV void async_ld64_sc1(u64& d, const void* p) { asm volatile("global_load_dwordx2 %0, %1, off sc1" : "=v"(d) : "v"(p) : "memory"); }
template <int N> DEV void wait_vm_for(u64& d) { asm volatile("s_waitcnt vmcnt(%1)" : "+v"(d) : "i"(N) : "memory"); }
template <int N> DEV void wait_vm() { asm volatile("s_waitcnt vmcnt(%0)" ::"i"(N) : "memory"); }
DEV void wait_lgkm0() { asm volatile("s_waitcnt lgkmcnt(0)" ::: "memory"); }
DEV void bar() { asm volatile("s_barrier" ::: "memory"); }
#define SBAR() __builtin_amdgcn_sched_barrier(0)
DEV u32 opq(u32 v) { asm volatile("" : "+v"(v)); return v; }
DEV bf16x8 lds128(u32 a) { return *(const LAS bf16x8*)a; }
DEV f32x4 lds128f(u32 a) { return *(const LAS f32x4*)a; }
typedef short v4i16_t __attribute__((ext_vector_type(4)));
DEV s16x4 ldtr(u32 a) { return __builtin_bit_cast(s16x4, __builtin_amdgcn_ds_read_tr16_b64_v4i16((LAS v4i16_t*)a)); }
DEV bf16x8 ldtr2(u32 a0, u32 a1) { s16x4 lo = ldtr(a0), hi = ldtr(a1); return __builtin_shufflevector(lo, hi, 0, 1, 2, 3, 4, 5, 6, 7); }
DEV f32x4 mfma(bf16x8 a, bf16x8 b, f32x4 c) { return __builtin_amdgcn_mfma_f32_16x16x32_bf16(a, b, c, 0, 0, 0); }
DEV void mfma_acc(f32x4& c, bf16x8 a, bf16x8 b) { asm volatile("v_mfma_f32_16x16x32_bf16 %0, %1, %2, %0" : "+v"(c) : "v"(a), "v"(b)); }
DEV void mfma_new(f32x4& c, bf16x8 a, bf16x8 b) { asm volatile("v_mfma_f32_16x16x32_bf16 %0, %1, %2, 0" : "=&v"(c) : "v"(a), "v"(b)); }
DEV void mfma_fence() { SBAR(); asm volatile("s_nop 7\n\ts_nop 1" ::: "memory"); SBAR(); }
typedef __attribute__((ext_vector_type(2))) __bf16 bf16x2_t;
DEV u32 pk(float lo, float hi) { bf16x2_t v = {(__bf16)lo, (__bf16)hi}; return __builtin_bit_cast(u32, v); }
DEV float bflo(u32 v) { return __uint_as_float(v << 16); }
DEV float bfhi(u32 v) { return __uint_as_float(v & 0xffff0000u); }
DEV float rbf(float f) { return bflo(pk(f, 0.f)); }
template <int CTRL> DEV float dppf(float v) { return __int_as_float(__builtin_amdgcn_update_dpp(0, __float_as_int(v), CTRL, 0xf, 0xf, true)); }
DEV float wave_sum(float v) {
  v += dppf<0x128>(v); v += dppf<0x124>(v); v += dppf<0x122>(v); v += dppf<0x121>(v);
  int i = __float_as_int(v);
  return (__int_as_float(__builtin_amdgcn_readlane(i, 0)) + __int_as_float(__builtin_amdgcn_readlane(i, 16))) +
         (__int_as_float(__builtin_amdgcn_readlane(i, 32)) + __int_as_float(__builtin_amdgcn_readlane(i, 48)));
}
DEV u64 ld64_sc1(const void* p) { return __hip_atomic_load((const __attribute__((address_space(1))) u64*)p, __ATOMIC_RELAXED, __HIP_MEMORY_SCOPE_AGENT); }
DEV void st64_sc1(void* p, u64 v) { __hip_atomic_store((__attribute__((address_space(1))) u64*)p, v, __ATOMIC_RELAXED, __HIP_MEMORY_SCOPE_AGENT); }
DEV __amdgpu_buffer_rsrc_t rsrc(const void* p, u32 bytes) { return __builtin_amdgcn_make_buffer_rsrc((void*)p, 0, bytes, 0x00020000); }

#define STAMP(i) do {} while (0)
#define STAMP_FLUSH() do {} while (0)

enum { IT_X, IT_W, IT_D, IT_O, IT_S };
struct Sched {
  int n = 0; int kind[128] = {}, idx[128] = {}, after[128] = {}, need[128] = {}, lds[128] = {}, cnt[128] = {};
  constexpr void add(int k, int i, int a, int nd, int l, int c) { kind[n] = k; idx[n] = i; after[n] = a; need[n] = nd; lds[n] = l; cnt[n] = c; n++; }
  constexpr int allowed(int b) const {
    int last = -1; for (int i = 0; i < n; i++) if (after[i] < b && need[i] <= b) last = i;
    int c = 0; for (int i = last + 1; i < n; i++) if (after[i] < b) c += cnt[i];
    return c;
  }
  constexpr int after_item(int it, int b) const { int c = 0; for (int i = it + 1; i < n; i++) if (after[i] < b) c += cnt[i]; return c; }
  constexpr int find(int k, int i) const { for (int j = 0; j < n; j++) if (kind[j] == k && idx[j] == i) return j; return -1; }
};
constexpr int HB_P[7] = {3, 4, 6, 7, 9, 10, 12};
constexpr int hb_unit(int i, int u) { return (u < 2) ? 2 * i + u : 14 + 2 * i + (u - 2); }
constexpr int cu_lds(int u) {
  using C = Cfg<true>;
  return u < 5 ? C::XSLOT + u * 4096 : u < 9 ? C::LW + 2 * C::WSLOT + (u - 5) * 4096 : u < 10 ? C::LW + 3 * C::WSLOT
       : u < 15 ? 2 * C::XSLOT + (u - 10) * 4096 : u < 19 ? C::LW + (u - 15) * 4096 : u < 24 ? (u - 19) * 4096 : C::LW + C::WSLOT + (u - 24) * 4096;
}
constexpr int cu_after(int u) { return u < 9 ? 10 : u < 10 ? 11 : u < 19 ? 12 : 13; }
constexpr int wo_after(int u) { return (u >= 3 && u < 6) ? 11 : (u >= 10) ? 13 : 15; }
template <bool FFN> constexpr Sched make_sched() {
  using C = Cfg<FFN>;
  Sched s;
  auto X = [&](int i, int a) { s.add(IT_X, i, a, 2 * i - 1, C::LX + (i % 3) * C::XSLOT, C::XP); };
  auto W = [&](int i, int a) { s.add(IT_W, i, a, i - 1, C::LW + (i % C::NWS) * C::WSLOT, C::WP); };
  W(0, -2); X(0, -2); W(1, -2); W(2, -2); X(1, -2); if (C::NWS == 4) W(3, -2); X(2, -2);
  for (int b = -1; b <= 15; b++) {
    if (b >= 0 && b + C::NWS < 14) W(b + C::NWS, b);
    if (b >= 2 && (b & 1) == 0 && b / 2 + 2 < 7) X(b / 2 + 2, b);
    if (b == 10) s.add(IT_S, 0, b, 99, 0, 1);
    if (FFN) {
      for (int i = 0; i < 7; i++) if (HB_P[i] == b) for (int u = 0; u < 4; u++) s.add(IT_D, hb_unit(i, u), b, b + 2, L_EB + ((i & 1) * 4 + u) * 4096, 1);
      for (int u = 0; u < 28; u++) if (cu_after(u) == b) s.add(IT_D, 28 + u, b, 14, cu_lds(u), 1);
      for (int u = 0; u < 14; u++) if (wo_after(u) == b) s.add(IT_O, u, b, 99, L_WO + u * 4096, 1);
    }
  }
  return s;
}

struct Args { const u16 *x, *wqkv, *wo, *wg, *wu, *wd; u16* out; char* ws; };

template <bool FFN> DEV void gemm1(f32x4 (&acc)[Cfg<FFN>::NRB][4], const u32 (&wa)[4], int lane) {
  using C = Cfg<FFN>;
  constexpr int NRB = C::NRB;
  const int j = lane & 15, g = lane >> 4;
  const u32 xa0 = j * 128 + ((g ^ (j >> 1)) << 4);
  const u32 xdl = (j == 15) ? (NRB - C::ONES_RB) * 2048 - 14 * 128 : 0;
  u32 xa[2], wb[4];
  for (int i = 0; i < (FFN ? 2 : 4); i++) wb[i] = opq(wa[i] + C::LW);
  auto ldx = [&](auto kc, auto rc) INL { constexpr int K = kc, R = rc;
    if constexpr (R == 0) xa[K & 1] = opq((xa0 ^ ((K & 1) << 6)) + C::LX + ((K >> 1) % 3) * C::XSLOT);
    return lds128((R == C::ONES_RB ? xa[K & 1] + xdl : xa[K & 1]) + R * 2048); };
  auto ldw = [&](auto kc, auto fc) INL { constexpr int K = kc, Fi = fc;
    constexpr int o = (K % C::NWS) * C::WSLOT + (FFN ? (Fi >> 1) * 8192 : 0);
    u32 a = FFN ? wb[Fi & 1] : wb[Fi];
    return ldtr2(a + o, a + o + 1024); };
  bf16x8 xb[4], wf[2][4];
  using I0 = std::integral_constant<int, 0>;
  sfor<4>([&](auto f) INL { wf[0][f] = ldw(I0{}, f); if constexpr (f < 3) xb[f] = ldx(I0{}, f); });
  SBAR();
  sfor<14>([&](auto kc) INL { constexpr int K = kc;
    sfor<NRB>([&](auto rc) INL { constexpr int R = rc;
      if constexpr (R == NRB - 4 && K < 13) { bar(); }
      using KN = std::integral_constant<int, K + 1>;
      if constexpr (R + 3 < NRB) xb[(K * NRB + R + 3) & 3] = ldx(kc, std::integral_constant<int, R + 3>{});
      else if constexpr (K < 13) xb[(K * NRB + R + 3) & 3] = ldx(KN{}, std::integral_constant<int, R + 3 - NRB>{});
      if constexpr (K < 13 && R >= NRB - 4) wf[(K + 1) & 1][R - (NRB - 4)] = ldw(KN{}, std::integral_constant<int, R - (NRB - 4)>{});
      sfor<4>([&](auto f) INL {
        if constexpr (K == 0) mfma_new(acc[R][f], wf[0][f], xb[R & 3]);
        else mfma_acc(acc[R][f], wf[K & 1][f], xb[(K * NRB + R) & 3]); });
      SBAR();
    });
  });
  mfma_fence();
}

DEV u32 w2_lane_addr(int lane, int cb, int row0) {
  const int g = lane >> 4, q = (lane >> 2) & 3, p = lane & 3, row = row0 + 4 * g + q;
  return row * 128 + ((cb ^ ((row >> 1) & 3)) << 5) + p * 8;
}

template <bool HELPER>
DEV void stage2(const Args& A, bf16x8 (&wd)[14][2], int wv, int lane, int rt, int ct, u64 tag, int vm_pending
) {
  char* ws = A.ws;
  const int j = lane & 15, g = lane >> 4;
  const int ch = wv & 1, kq2 = wv >> 1, kq = (HELPER ? 0 : 2) + kq2;
  constexpr int NO = HELPER ? 3 : 2, RB0 = HELPER ? 0 : 3;
  const int cb = 2 * ch + kq2;
  const srd_t sact = make_srd(ws + WS_ACT + (size_t)rt * ACT_TILE, ACT_TILE);
  const u32 vlin = lane * 16;
  const u32 lpart = wv * 5120;
  const u32 sf = wv * 14 * 5120, so = (56 + wv) * 5120;
  auto slot = [](int r) constexpr { return ((r + 1) % 5) * RSLOT; };
  auto issue = [&](auto rc) INL { constexpr int R = rc;
    sfor<5>([&](auto p) INL {
      if constexpr (R < 14) dma_sc1<slot(R) + p * 1024, R * 5120 + p * 1024>(vlin, sact, sf, lpart);
      else dma_sc1<slot(R) + p * 1024, (R - 14) * 4 * 5120 + p * 1024>(vlin, sact, so, lpart); }); };
  u32x2 xr[NO];
  const srd_t sx = make_srd(A.x, S_ * XROWB);
  u32 ooff[NO];
  for (int i = 0; i < NO; i++) {
    const int lr = (RB0 + i) * 16 + j, row = rt * T2ROWS + lr;
    ooff[i] = (lr < T2ROWS && row < S_) ? (u32)(row * XROWB + ct * 128 + cb * 32 + g * 8) : 0x80000000u;
    asm volatile("buffer_load_dwordx2 %0, %1, %2, 0 offen" : "=v"(xr[i]) : "v"(ooff[i]), "s"(sx) : "memory");
  }
  if constexpr (HELPER) {
    const char* fp = ws + WS_HFLAG + (rt >> 1) * 512 + (wv * 14 + (lane < 14 ? lane : 0)) * 8;
    for (u32 sp = 0; sp < SPIN_CAP; sp++) { u64 v = ld64_sc1(fp); if (__all(v == tag)) break; __builtin_amdgcn_s_sleep(1); }
    asm volatile("" ::: "memory");
    STAMP(7);
    sfor<5>([&](auto r) INL { issue(r); });
    wait_vm<20>();
  } else {
    wait_vm<0>();
  }
  for (int i = 0; i < NO; i++) asm volatile("" : "+v"(xr[i]));
  bar();
  STAMP(8);
  bf16x8 fr[2][6], wo_f[8][2];
  f32x4 af[5][2], as[NO][2], fo[NO];
  const u32 fa = kq * 5120 + vlin;
  const u32 sa = kq2 * 2 * 5120 + RB0 * 1024 + vlin;
  auto rd = [&](auto rc) INL { constexpr int R = rc;
    const u32 a = opq((R < 14 ? fa : sa) + slot(R));
    if constexpr (R < 14) sfor<5>([&](auto i) INL { fr[R & 1][i] = lds128(a + i * 1024); });
    else sfor<2 * NO>([&](auto i) INL { fr[R & 1][i] = lds128(a + (i / NO) * 5120 + (i % NO) * 1024); }); };
  rd(std::integral_constant<int, 0>{});
  u64 oflag = 0;
  const int tq0 = (rt * T2ROWS) / QROWS, tq1 = min(rt * T2ROWS + T2ROWS - 1, S_ - 1) / QROWS;
  const char* ofp = ws + WS_OFLAG + (((lane & 8) ? tq1 : tq0) * 8 + ((lane & 7) < 6 ? (lane & 7) : 0)) * 8;
  sfor<18>([&](auto rc) INL { constexpr int R = rc;
    if constexpr (R < 17) {
      if constexpr (HELPER) {
        constexpr int n = R + 1;
        if constexpr (n <= 4 || (n & 1) == 0) {
          constexpr int hi = (R + 4 > 17) ? 17 : R + 4;
          constexpr int later = [] { int c = 0; for (int m = n + 1; m <= hi; m++) if (m <= 4 || (m & 1) == 0) c++; return c; }();
          wait_vm<5 * later>();
        }
        if constexpr (R == 9) {
          for (u32 sp = 0; sp < SPIN_CAP; sp++) { oflag = ld64_sc1(ofp); if (__all(oflag == tag)) break; __builtin_amdgcn_s_sleep(1); }
          asm volatile("" ::: "memory");
        }
      } else {
        constexpr int n = R + 1;
        if constexpr (n > 4 && (n & 1) == 1) {
          constexpr int hi = (R + 4 > 17) ? 17 : R + 4;
          constexpr int later = [] { int c = 0; for (int m = n + 1; m <= hi; m++) if (m > 4 && (m & 1) == 1) c++; return c; }();
          wait_vm<5 * later>();
        }
      }
      wait_lgkm0();
      bar();
      rd(std::integral_constant<int, R + 1>{});
      if constexpr (R + 5 < 18 && (((R + 5) & 1) == 0) == HELPER) issue(std::integral_constant<int, R + 5>{});
    }
    if constexpr (R >= 5 && R < 13) {
      constexpr int i = R - 5;
      const int row0 = (4 * (i >> 1) + 2 * kq2 + (i & 1)) * 28;
      for (int c = 0; c < 2; c++) {
        const u32 a0 = w2_lane_addr(lane, 2 * ch + c, row0);
        const u32 a1 = w2_lane_addr(lane, 2 * ch + c, row0 + (g == 3 ? 12 : 16));
        wo_f[i][c] = ldtr2(L_WO + a0, L_WO + a1);
      }
    }
    if constexpr (R < 14) {
      sfor<5>([&](auto i) INL { sfor<2>([&](auto c) INL {
        if constexpr (R == 0) mfma_new(af[i][c], wd[R][c], fr[R & 1][i]);
        else mfma_acc(af[i][c], wd[R][c], fr[R & 1][i]); }); });
    } else {
      sfor<2 * NO>([&](auto i) INL { sfor<2>([&](auto c) INL {
        if constexpr (R == 14 && i < NO) mfma_new(as[i % NO][c], wo_f[(R - 14) * 2 + i / NO][c], fr[R & 1][i]);
        else mfma_acc(as[i % NO][c], wo_f[(R - 14) * 2 + i / NO][c], fr[R & 1][i]); }); });
    }
    if constexpr (R == 14) {
      SBAR();
      sfor<5>([&](auto i) INL { sfor<2>([&](auto c) INL {
        constexpr int okq = (i < 3 ? 0 : 2) + c;
        constexpr bool maybe_mine = (i >= RB0 && i < RB0 + NO);
        const int pos = (kq - okq - 1) & 3;
        if (!maybe_mine || pos != 3) *(LAS f32x4*)(L_FS + ((i * 4 + 2 * ch + c) * 3 + pos) * 1024 + vlin) = af[i][c];
      }); });
      sfor<NO>([&](auto i) INL { fo[i] = kq2 ? af[RB0 + i][1] : af[RB0 + i][0]; });
    }
    if constexpr (R == 16) {
      sfor<NO>([&](auto i) INL { sfor<3>([&](auto p) INL { fo[i] += lds128f(L_FS + (((RB0 + i) * 4 + cb) * 3 + p) * 1024 + vlin); }); });
    }
    SBAR();
  });
  mfma_fence();
  STAMP(9);
  constexpr int L_SS = slot(14);
  sfor<NO>([&](auto i) INL { *(LAS f32x4*)(L_SS + ((RB0 + i) * 4 + (cb ^ 1)) * 1024 + vlin) = kq2 ? as[i][0] : as[i][1]; });
  wait_lgkm0();
  bar();
  const __amdgpu_buffer_rsrc_t ro = rsrc(A.out, S_ * XROWB);
  sfor<NO>([&](auto i) INL {
    f32x4 sv = (kq2 ? as[i][1] : as[i][0]) + lds128f(L_SS + ((RB0 + i) * 4 + cb) * 1024 + vlin);
    float o[4];
    for (int r = 0; r < 4; r++) {
      const float f = rbf(fo[i][r]);
      const float gt = rbf(f * __builtin_amdgcn_rcpf(1.f + __builtin_amdgcn_exp2f(-1.4426950408889634f * f)));
      const float xv = (r & 1) ? bfhi(xr[i][r >> 1]) : bflo(xr[i][r >> 1]);
      o[r] = xv + rbf(rbf(sv[r]) * gt);
    }
    u32x2 ov = {pk(o[0], o[1]), pk(o[2], o[3])};
    __builtin_amdgcn_raw_buffer_store_b64(ov, ro, ooff[i], 0, 0);
  });
  STAMP(10);
}

template <bool FFN>
DEV void loader(const Args& A, bf16x8 (&wdf)[14][2], int hw, int lane, int tile, int grp, int ct
) {
  using C = Cfg<FFN>;
  static constexpr Sched SC = make_sched<FFN>();
  const srd_t sx = make_srd(A.x, S_ * XROWB);
  const srd_t sw = FFN ? make_srd((hw & 1) ? A.wu : A.wg, D_ * GROWB) : make_srd(A.wqkv, D_ * QROWB);
  const srd_t sd = make_srd(A.wd, F_ * XROWB), so = make_srd(A.wo, D_ * XROWB);
  auto grow = [&](int L) INL {
    int r = FFN ? tile * (2 * T2ROWS) + L - (L >= 80 ? 1 : 0) : tile * QROWS - 14 + L;
    return max(0, min(r, S_ - 1)); };
  u32 xv[C::XP];
  for (int i = 0; i < C::XP; i++) { const int L = (hw * C::XP + i) * 8 + (lane >> 3); xv[i] = grow(L) * XROWB + (((lane & 7) ^ ((L >> 1) & 7)) << 4); }
  constexpr int WROWB = FFN ? GROWB : QROWB;
  u32 wv[2], wsb, wlb;
  if constexpr (FFN) {
    for (int i = 0; i < 2; i++) wv[i] = (lane >> 4) * WROWB + (((lane & 15) ^ (((lane >> 4) | (i << 2)) << 1)) << 4);
    wsb = (hw >> 1) * 16 * WROWB + grp * 256; wlb = (hw & 1) * 8192 + (hw >> 1) * 4096;
  } else {
    wv[0] = wv[1] = (lane >> 4) * WROWB + (((lane & 15) ^ (((lane >> 4) | ((hw & 1) << 2)) << 1)) << 4);
    wsb = hw * 8 * WROWB + grp * 128; wlb = hw * 2048;
  }
  const u32 xlb = hw * C::XP * 1024;
  const u32 dv = (lane >> 3) * XROWB + (((lane & 7) ^ ((lane >> 4) << 1)) << 4);
  const u32 dsb = hw * 8 * XROWB + ct * 128, dlb = hw * 1024;
  const u32 zero = 0;
  u64 gran = 0;
  constexpr int SROWS = C::NRB * 4;
  const char* gp = A.ws + WS_STAT + grow(hw * SROWS + min(lane, SROWS - 1)) * 8;
  auto issue_after = [&](auto bc) INL { constexpr int B = bc;
    sfor<SC.n>([&](auto ic) INL { constexpr int I = ic;
      if constexpr (SC.after[I] == B) {
        constexpr int id = SC.idx[I], L = SC.lds[I];
        if constexpr (SC.kind[I] == IT_X) sfor<C::XP>([&](auto p) INL { dma<L + p * 1024, id * 128>(xv[p], sx, zero, xlb); });
        else if constexpr (SC.kind[I] == IT_W) sfor<C::WP>([&](auto p) INL {
          if constexpr (FFN) dma<L + p * 1024, (id * 32 + p * 4) * WROWB>(wv[p >> 1], sw, wsb, wlb);
          else dma<L + (p >> 1) * 8192 + (p & 1) * 1024, (id * 32 + (p & 1) * 4) * WROWB + (p >> 1) * XROWB>(wv[0], sw, wsb, wlb); });
        else if constexpr (SC.kind[I] == IT_D) dma<L, id * 32 * XROWB>(dv, sd, dsb, dlb);
        else if constexpr (SC.kind[I] == IT_O) dma<L, id * 32 * XROWB>(dv, so, dsb, dlb);
        else async_ld64_sc1(gran, gp);
      } }); };
  issue_after(std::integral_constant<int, -2>{});
  STAMP(1);
  const u32 da0 = opq(w2_lane_addr(lane, 2 * (hw & 1), 0) + L_EB + (hw >> 1) * 8192), da1 = opq(w2_lane_addr(lane, 2 * (hw & 1) + 1, 0) + L_EB + (hw >> 1) * 8192);
  constexpr int LASTB = FFN ? 15 : 13;
  sfor<LASTB + 2>([&](auto bc) INL { constexpr int B = bc - 1;
    if constexpr (B == 12) {
      wait_vm_for<SC.after_item(SC.find(IT_S, 0), B)>(gran);
      const u32 tag = *(volatile LAS u32*)L_TAG;
      for (u32 sp = 0; sp < SPIN_CAP && !__all((u32)(gran >> 32) == tag); sp++) { __builtin_amdgcn_s_sleep(1); gran = ld64_sc1(gp); }
      f32x2 st = {bflo((u32)gran), 1.0f / bfhi((u32)gran)};
      if (lane < SROWS) *(LAS f32x2*)(L_STAT + (hw * SROWS + lane) * 8) = st;
    }
    wait_vm<SC.allowed(B)>();
    wait_lgkm0();
    bar();
    if constexpr (B == -1) STAMP(2);
    if constexpr (B == 13) STAMP(3);
    if constexpr (B == 14) STAMP(5);
    issue_after(std::integral_constant<int, B>{});
    if constexpr (FFN) sfor<7>([&](auto hc) INL { constexpr int H = hc;
      if constexpr (HB_P[H] + 2 == B) sfor<2>([&](auto u) INL {
        constexpr int o = ((H & 1) * 4 + u) * 4096;
        wdf[2 * H + u][0] = ldtr2(da0 + o, da0 + o + 2048); wdf[2 * H + u][1] = ldtr2(da1 + o, da1 + o + 2048); }); });
    SBAR();
  });
}

__global__ void __launch_bounds__(512, 2) fused_kernel(Args A) {
  __shared__ __attribute__((aligned(16))) char smem[LDS_BYTES];
  asm volatile("" ::"s"((u32)(size_t)(LAS char*)smem));
  const int bid = blockIdx.x, lane = threadIdx.x & 63;
  const int wid = __builtin_amdgcn_readfirstlane(threadIdx.x >> 6);
  char* ws = A.ws;
#define SARG
  STAMP(0);
  const int xcd = bid & 7, r = bid >> 3, rg = xcd >> 1, chf = xcd & 1;
  const bool is_qkv = r < 9;
  const int fi = rg * 23 + r - 9;
  const bool idle = !is_qkv && fi >= 91;
  const int tile = is_qkv ? rg * 3 + r / 3 : fi / 7;
  const int grp = is_qkv ? chf * 3 + r % 3 : chf * 7 + fi % 7;
  const int j2 = xcd * 23 - (xcd == 7 ? 1 : 0) + r - 9;
  const int rt = j2 / 7, ct = j2 % 7;
  const int j = lane & 15, g = lane >> 4;

  if (wid >= 4) {
    const int hw = wid - 4;
    if (idle) { STAMP_FLUSH(); return; }
    if (!is_qkv) {
      bf16x8 wdf[14][2];
      loader<true>(A, wdf, hw, lane, tile, grp, ct SARG);
      STAMP(6);
      const u64 tag = *(volatile LAS u64*)L_TAG;
      stage2<true>(A, wdf, hw, lane, rt, ct, tag, 0 SARG);
      STAMP_FLUSH();
      return;
    }
    bf16x8 dummy[14][2];
    loader<false>(A, dummy, hw, lane, tile, grp, 0 SARG);
  } else {
    const int cw = wid;
    const u64 tag = ld64_sc1(ws + WS_EPOCH) + 1;
    {
      const __amdgpu_buffer_rsrc_t rx = rsrc(A.x, S_ * XROWB);
      u32x4 v[2];
      for (int i = 0; i < 2; i++) v[i] = __builtin_bit_cast(u32x4, __builtin_amdgcn_raw_buffer_load_b128(rx, lane < 56 ? (bid * 8 + cw * 2 + i) * XROWB + lane * 16 : 0x80000000u, 0, 0));
      if (!idle && cw < 3 && lane < 16) *(LAS u32x4*)((is_qkv ? Cfg<false>::XSLOT : Cfg<true>::XSLOT) * (cw + 1) - 256 + lane * 16) = u32x4{0x3f803f80u, 0x3f803f80u, 0x3f803f80u, 0x3f803f80u};
      u32 res[2];
      for (int i = 0; i < 2; i++) {
        float e[8]; float s = 0.f;
        for (int c = 0; c < 4; c++) { e[2 * c] = bflo(v[i][c]); e[2 * c + 1] = bfhi(v[i][c]); s += e[2 * c] + e[2 * c + 1]; }
        const float mean = wave_sum(s) * (1.0f / D_);
        float q = 0.f;
        for (int c = 0; c < 8; c++) { const float d = e[c] - mean; q += d * d; }
        if (lane >= 56) q = 0.f;
        const float var = rbf(wave_sum(q) * (1.0f / D_));
        res[i] = pk(mean, sqrtf(rbf(var + 1e-5f)));
      }
      if (lane < 2) st64_sc1(ws + WS_STAT + (bid * 8 + cw * 2 + lane) * 8, (tag << 32) | (lane ? res[1] : res[0]));
      if (cw == 0 && lane == 0) *(LAS u64*)L_TAG = tag;
    }
    if (idle) {
      if (xcd == 7) {
        for (u32 sp = 0; sp < SPIN_CAP; sp++) {
          bool ok = true;
          if (cw == 0) for (int i = 0; i < 16; i++) ok &= (u32)(ld64_sc1(ws + WS_STAT + ((i * 64 + lane) * 2) * 8) >> 32) == (u32)tag;
          if (__all(ok)) break;
          __builtin_amdgcn_s_sleep(8);
        }
        if (cw == 0 && lane == 0) st64_sc1(ws + WS_EPOCH, tag);
      }
      STAMP_FLUSH();
      return;
    }
    STAMP(1);
    wait_lgkm0();
    bar();
    STAMP(2);
    const int q = (lane >> 2) & 3, p = lane & 3;
    const u32 wrow = (8 * g + q) * 256, wsw = (q | ((g & 1) << 2)) << 2;
    if (!is_qkv) {
      f32x4 acc[10][4];
      u32 wa[4];
      for (int f = 0; f < 2; f++) wa[f] = wrow + (((8 * cw + 4 * f + p) ^ wsw) << 3);
      wa[2] = wa[3] = 0;
      gemm1<true>(acc, wa, lane);
      bar();
      STAMP(3);
      f32x4 cs[4];
      for (int f = 0; f < 4; f++) for (int e = 0; e < 4; e++) cs[f][e] = dppf<0x15f>(acc[4][f][e]);
      const __amdgpu_buffer_rsrc_t ra = rsrc(ws + WS_ACT + (size_t)(2 * tile) * ACT_TILE, 2 * ACT_TILE);
      const u32 hb = (grp * 4 + cw) * 5120 + lane * 16, sta = opq(L_STAT + j * 8);
      sfor<10>([&](auto rc) INL { constexpr int R = rc;
        const f32x2 st = *(const LAS f32x2*)(sta + R * 128);
        const float r2 = st[1] * st[1];
        f32x4 h0 = (acc[R][0] - st[0] * cs[0]) * (acc[R][2] - st[0] * cs[2]) * r2;
        f32x4 h1 = (acc[R][1] - st[0] * cs[1]) * (acc[R][3] - st[0] * cs[3]) * r2;
        u32x4 o = {pk(h0[0], h0[1]), pk(h0[2], h0[3]), pk(h1[0], h1[1]), pk(h1[2], h1[3])};
        __builtin_amdgcn_raw_buffer_store_b128(o, ra, hb, (R / 5) * ACT_TILE + (R % 5) * 1024, 16);
      });
      STAMP(4);
      bar();
      bf16x8 wdf[14][2];
      {
        u32 da[2][2];
        for (int c = 0; c < 2; c++) { da[0][c] = opq(w2_lane_addr(lane, 2 * (cw & 1) + c, 0)); da[1][c] = opq(da[0][c] + 61440); }
        auto drain = [&](auto bc) INL { sfor<14>([&](auto u) INL { constexpr int o = cu_lds(bc + u), hi = o >= 61440, oo = o - hi * 61440;
          wdf[u][0] = ldtr2(da[hi][0] + oo, da[hi][0] + oo + 2048); wdf[u][1] = ldtr2(da[hi][1] + oo, da[hi][1] + oo + 2048); }); };
        if (cw >> 1) drain(std::integral_constant<int, 14>{}); else drain(std::integral_constant<int, 0>{});
      }
      wait_vm<0>();
      if (lane == 0) st64_sc1(ws + WS_HFLAG + tile * 512 + (grp * 4 + cw) * 8, tag);
      STAMP(5);
      wait_lgkm0();
      bar();
      STAMP(6);
      stage2<false>(A, wdf, cw, lane, rt, ct, tag, 0 SARG);
      STAMP_FLUSH();
      return;
    }
    f32x4 acc[12][4];
    u32 wa[4];
    const int h0 = grp < 3 ? 3 * grp : (grp == 3 ? 8 : grp == 4 ? 10 : 13);
    const int Q0 = 7 * h0 - 16 * grp;
    for (int f = 0; f < 4; f++) {
      int id = min(16 * cw + 4 * f + p, 62), m = id / 21;
      wa[f] = m * 8192 + wrow + (((Q0 + id - 21 * m) ^ wsw) << 3);
    }
    gemm1<false>(acc, wa, lane);
    bar();
    STAMP(3);
    f32x4 cs[4];
    for (int f = 0; f < 4; f++) for (int e = 0; e < 4; e++) cs[f][e] = dppf<0x15f>(acc[11][f][e]);
    const u32 sta = opq(L_STAT + j * 8);
    u32 io[4];
    for (int f = 0; f < 4; f++) { const int id = 16 * cw + 4 * f + (opq(lane) >> 4); io[f] = j * IMGROW + (id / 7) * 64 + (id % 7) * 8; }
    sfor<12>([&](auto rc) INL { constexpr int R = rc;
      const f32x2 st = *(const LAS f32x2*)(sta + R * 128);
      if constexpr (R == 6) for (int f = 0; f < 4; f++) io[f] = opq(io[f] + 6 * 16 * IMGROW);
      sfor<4>([&](auto f) INL {
        const f32x4 v = (acc[R][f] - st[0] * cs[f]) * st[1];
        *(LAS u32x2*)(io[f] + (R % 6) * 16 * IMGROW) = u32x2{pk(v[0], v[1]), pk(v[2], v[3])};
      });
    });
    STAMP(4);
  }
  wait_lgkm0();
  bar();
  STAMP(5);
  {
    const u64 tag = *(volatile LAS u64*)L_TAG;
    const int h0 = grp < 3 ? 3 * grp : (grp == 3 ? 8 : grp == 4 ? 10 : 13);
    const int hh0 = (grp == 3 || grp == 4) ? 1 : 0;
    const __amdgpu_buffer_rsrc_t ra = rsrc(ws + WS_ACT, 26 * ACT_TILE);
    const int par = j & 1, q = (lane >> 2) & 3, p = lane & 3;
    const u32 pm = par ? 0xffff0000u : 0x0000ffffu;
    const u32 padm = (g == 3) ? 0u : 0xffffffffu;
    for (int it = hh0 * 11 + wid; it < 33; it += 8) {
      const int hh = it / 11, b = it - hh * 11;
      const u32 rowa = (16 * b + j) * IMGROW + hh * 64 + g * 16;
      u32x4 qf = __builtin_bit_cast(u32x4, lds128(rowa + 14 * IMGROW));
      u32x4 k0 = __builtin_bit_cast(u32x4, lds128(rowa + 192)), k1 = __builtin_bit_cast(u32x4, lds128(rowa + 192 + 16 * IMGROW));
      const u32 va = (16 * b + 4 * g + q) * IMGROW + 384 + hh * 64 + p * 8;
      const bf16x8 v0 = ldtr2(va, va + 16 * IMGROW), v1 = ldtr2(va + 32, va + 32 + 16 * IMGROW);
      qf[2] &= padm; qf[3] &= padm; k0[2] &= padm; k0[3] &= padm; k1[2] &= padm; k1[3] &= padm;
      const f32x4 z = {0.f, 0.f, 0.f, 0.f};
      const f32x4 s0 = mfma(__builtin_bit_cast(bf16x8, k0), __builtin_bit_cast(bf16x8, qf), z);
      const f32x4 s1 = mfma(__builtin_bit_cast(bf16x8, k1), __builtin_bit_cast(bf16x8, qf), z);
      float l[4] = {par ? s0[1] : s0[0], par ? s0[3] : s0[2], par ? s1[1] : s1[0], par ? s1[3] : s1[2]};
      const int kg0 = tile * QROWS - 14 + 16 * b;
      float mx = -3.0e38f;
      for (int c = 0; c < 4; c++) {
        const int ki = 16 * (c >> 1) + 4 * g + 2 * (c & 1) + par, dl = 14 + j - ki;
        float v = rbf(rbf(l[c]) * 0.18898223650461362f);
        if (kg0 + ki < 0) v = -1.0e9f;
        l[c] = (dl >= 0 && dl <= 14) ? v : -3.0e38f;
        mx = fmaxf(mx, l[c]);
      }
      mx = fmaxf(mx, __shfl_xor(mx, 16)); mx = fmaxf(mx, __shfl_xor(mx, 32));
      float sum = 0.f;
      for (int c = 0; c < 4; c++) { l[c] = __builtin_amdgcn_exp2f((l[c] - mx) * 1.4426950408889634f); sum += l[c]; }
      sum += __shfl_xor(sum, 16); sum += __shfl_xor(sum, 32);
      const float rs = __builtin_amdgcn_rcpf(sum);
      u32x4 pf;
      for (int c = 0; c < 4; c++) { const float pv = l[c] * rs; pf[c] = pk(pv, pv) & pm; }
      const f32x4 o0 = mfma(v0, __builtin_bit_cast(bf16x8, pf), z), o1 = mfma(v1, __builtin_bit_cast(bf16x8, pf), z);
      const u32x4 ov = {pk(o0[0], o0[1]), pk(o0[2], o0[3]), pk(o1[0], o1[1]) & padm, pk(o1[2], o1[3]) & padm};
      const int lq = 16 * b + j, row = tile * QROWS + lq;
      const int t2 = row / T2ROWS, lr = row - t2 * T2ROWS;
      const u32 off = (lq < QROWS && row < S_) ? (u32)(t2 * ACT_TILE + (56 + h0 + hh) * 5120 + (lr >> 4) * 1024 + (g * 16 + (lr & 15)) * 16) : 0x80000000u;
      __builtin_amdgcn_raw_buffer_store_b128(ov, ra, off, 0, 16);
    }
    STAMP(6);
    wait_vm<0>();
    bar();
    if (wid == 0 && lane == 0) st64_sc1(ws + WS_OFLAG + (tile * 8 + grp) * 8, tag);
    STAMP(7);
    STAMP_FLUSH();
  }
}

extern "C" void kernel_launch(void* const* d_in, const int* in_sizes, int n_in, void* d_out, int out_size, void* d_ws, size_t ws_size, hipStream_t stream) {
  Args A;
  A.x = (const u16*)d_in[0]; A.wqkv = (const u16*)d_in[1]; A.wo = (const u16*)d_in[2];
  A.wg = (const u16*)d_in[4]; A.wu = (const u16*)d_in[5]; A.wd = (const u16*)d_in[6];
  A.out = (u16*)d_out; A.ws = (char*)d_ws;
  fused_kernel<<<256, 512, 0, stream>>>(A);
}
```

```cpp
#include <hip/hip_runtime.h>
#pragma clang diagnostic ignored "-Wint-to-pointer-cast"
#include <utility>

typedef unsigned short u16;
typedef unsigned int u32;
typedef unsigned long long u64;
typedef __attribute__((ext_vector_type(8))) short bf16x8;
typedef __attribute__((ext_vector_type(4))) short s16x4;
typedef __attribute__((ext_vector_type(4))) float f32x4;
typedef __attribute__((ext_vector_type(2))) float f32x2;
typedef __attribute__((ext_vector_type(4))) u32 u32x4;
typedef __attribute__((ext_vector_type(2))) u32 u32x2;
typedef __attribute__((ext_vector_type(4))) int srd_t;
#define LAS __attribute__((address_space(3)))
#define DEV __device__ __forceinline__
#define INL __attribute__((always_inline))

template <class F, int... I> DEV void sfor_impl(F&& f, std::integer_sequence<int, I...>) { (f(std::integral_constant<int, I>{}), ...); }
template <int N, class F> DEV void sfor(F&& f) { sfor_impl(f, std::make_integer_sequence<int, N>{}); }

constexpr int S_ = 2048, D_ = 448, F_ = 1792;
constexpr int XROWB = D_ * 2;
constexpr int GROWB = F_ * 2;
constexpr int QROWB = 3 * D_ * 2;
constexpr int T2ROWS = 79;
DEV int blk_row(int B) { return 16 * B - ((B * 205) >> 10); }
constexpr int WS_EPOCH = 0;
constexpr int FSTR = 128;
constexpr int WS_HFLAG = 65536;
constexpr int WS_OFLAG = 4096;
constexpr int WS_STAT = 32768;
constexpr int WS_ACT = 98304;
constexpr int ACT_TILE = 72 * 5120;
constexpr int LDS_BYTES = 163840;
constexpr int L_STAT = 160512;
constexpr int L_DENW = 159232;
constexpr int L_TAG = 162048;
constexpr int L_ZERO = 162064;
constexpr int L_ROWS = 148224;
constexpr int L_JUNK = 157440;
template <bool FFN> struct Cfg {
  static constexpr int NRB = FFN ? 10 : 12;
  static constexpr int NMAT = FFN ? 2 : 3;
  static constexpr int XSLOT = NRB * 2048 + 256;
  static constexpr int NWS = FFN ? 4 : 3;
  static constexpr int WSLOT = NMAT * 8192;
  static constexpr int LX = 0, LW = 3 * XSLOT;
  static constexpr int XP = NRB / 2;
  static constexpr int WP = NMAT * 2;
  static constexpr int ONES_RB = FFN ? 4 : 11;
};
constexpr int L_EB = 127744;
constexpr int L_WO = 103168;
constexpr int RSLOT = 20480;
constexpr int IMGROW = 592;

constexpr int L_JUNK2 = 161280;
constexpr int CODE_KB = 35;
constexpr int WK0 = 96, WK1 = 256;
constexpr u32 SPIN_CAP = 1u << 18;

DEV srd_t make_srd(const void* p, u32 bytes) {
  u64 a = (u64)p; srd_t s; s[0] = (int)(u32)a; s[1] = (int)((u32)(a >> 32) & 0xffffu); s[2] = (int)bytes; s[3] = 0x00020000; return s;
}
template <int LI, int SI> DEV u32 dma_set(u32 sb, u32 lb) {
  u32 t; asm volatile("s_add_u32 m0, %2, %3\n\ts_add_u32 %0, %1, %4" : "=&s"(t) : "s"(sb), "s"(lb), "i"(LI), "i"(SI) : "memory", "scc"); return t;
}
template <int OFF> DEV void dma(u32 voff, srd_t srd, u32 so) {
  asm volatile("buffer_load_dwordx4 %0, %1, %2 offen offset:%3 lds" :: "v"(voff), "s"(srd), "s"(so), "i"(OFF) : "memory");
}
template <int OFF> DEV void dma_sc1(u32 voff, srd_t srd, u32 so) {
  asm volatile("buffer_load_dwordx4 %0, %1, %2 offen offset:%3 sc1 lds" :: "v"(voff), "s"(srd), "s"(so), "i"(OFF) : "memory");
}
DEV void async_ld64_sc1(u64& d, const void* p) { asm volatile("global_load_dwordx2 %0, %1, off sc1" : "=v"(d) : "v"(p) : "memory"); }
DEV void async_bld64(u32x2& d, u32 off, srd_t srd) { asm volatile("buffer_load_dwordx2 %0, %1, %2, 0 offen" : "=v"(d) : "v"(off), "s"(srd) : "memory"); }
DEV const void* karg(int i) { return *(const void* const __attribute__((address_space(4)))*)((const __attribute__((address_space(4))) char*)__builtin_amdgcn_kernarg_segment_ptr() + i * 8); }
DEV void touch_at(const void* p, u32 off) { asm volatile("s_mov_b32 m0, %2\n\ts_nop 0\n\tglobal_load_lds_dword %0, %1" :: "v"(off), "s"(p), "i"(L_JUNK2) : "memory"); }
DEV void dma_word(const void* p, u32 lds) { asm volatile("s_mov_b32 m0, %1\n\ts_nop 0\n\tglobal_load_lds_dword %0, off sc1" :: "v"(p), "s"(lds) : "memory"); }
DEV void pinp(const char*& p) { asm volatile("" : "+v"(p)); }
DEV void pins(int& v) { asm volatile("" : "+s"(v)); }
template <int N> DEV void wait_vm_for(u64& d) { asm volatile("s_waitcnt vmcnt(%1)" : "+v"(d) : "i"(N) : "memory"); }
template <int N> DEV void wait_vm() { asm volatile("s_waitcnt vmcnt(%0)" ::"i"(N) : "memory"); }
DEV void wait_lgkm0() { asm volatile("s_waitcnt lgkmcnt(0)" ::: "memory"); }
DEV void bar() { asm volatile("s_barrier" ::: "memory"); }
#define SBAR() __builtin_amdgcn_sched_barrier(0)
DEV u32 opq(u32 v) { asm volatile("" : "+v"(v)); return v; }
DEV bf16x8 lds128(u32 a) { return *(const LAS bf16x8*)a; }
DEV f32x4 lds128f(u32 a) { return *(const LAS f32x4*)a; }
typedef short v4i16_t __attribute__((ext_vector_type(4)));
DEV s16x4 ldtr(u32 a) { return __builtin_bit_cast(s16x4, __builtin_amdgcn_ds_read_tr16_b64_v4i16((LAS v4i16_t*)a)); }
DEV bf16x8 ldtr2(u32 a0, u32 a1) { s16x4 lo = ldtr(a0), hi = ldtr(a1); return __builtin_shufflevector(lo, hi, 0, 1, 2, 3, 4, 5, 6, 7); }
DEV f32x4 mfma(bf16x8 a, bf16x8 b, f32x4 c) { return __builtin_amdgcn_mfma_f32_16x16x32_bf16(a, b, c, 0, 0, 0); }
DEV void mfma_acc(f32x4& c, bf16x8 a, bf16x8 b) { asm volatile("v_mfma_f32_16x16x32_bf16 %0, %1, %2, %0" : "+v"(c) : "v"(a), "v"(b)); }
DEV void mfma_new(f32x4& c, bf16x8 a, bf16x8 b) { asm volatile("v_mfma_f32_16x16x32_bf16 %0, %1, %2, 0" : "=&v"(c) : "v"(a), "v"(b)); }
DEV void mfma_fence() { SBAR(); asm volatile("s_nop 7\n\ts_nop 1" ::: "memory"); SBAR(); }
typedef __attribute__((ext_vector_type(2))) __bf16 bf16x2_t;
DEV u32 pk(float lo, float hi) { bf16x2_t v = {(__bf16)lo, (__bf16)hi}; return __builtin_bit_cast(u32, v); }
DEV float bflo(u32 v) { return __uint_as_float(v << 16); }
DEV float bfhi(u32 v) { return __uint_as_float(v & 0xffff0000u); }
DEV float rbf(float f) { return bflo(pk(f, 0.f)); }
template <int CTRL> DEV float dppf(float v) { return __int_as_float(__builtin_amdgcn_update_dpp(0, __float_as_int(v), CTRL, 0xf, 0xf, true)); }
DEV u64 ld64_sc1(const void* p) { return __hip_atomic_load((const __attribute__((address_space(1))) u64*)p, __ATOMIC_RELAXED, __HIP_MEMORY_SCOPE_AGENT); }
DEV void st64_sc1(void* p, u64 v) { __hip_atomic_store((__attribute__((address_space(1))) u64*)p, v, __ATOMIC_RELAXED, __HIP_MEMORY_SCOPE_AGENT); }
DEV __amdgpu_buffer_rsrc_t rsrc(const void* p, u32 bytes) { return __builtin_amdgcn_make_buffer_rsrc((void*)p, 0, bytes, 0x00020000); }

constexpr int F_WX[17] = { 18, 6, 9, 9, 9, 9, 9, 9, 9, 9, 9, 9, 9, 9, 9, 9, 9 };
constexpr int F_H[17]  = {  0, 0, 0, 1, 0, 2, 0, 2, 0, 2, 0, 2, 1, 0, 0, 2, 2 };
constexpr int F_C[17]  = {  0, 0, 0, 0, 0, 0, 0, 0, 0, 0, 0, 0, 7, 7,14, 0, 0 };
constexpr int F_O[17]  = {  0, 0, 0, 0, 0, 0, 0, 0, 0, 0, 0, 0, 0, 0, 0, 6, 8 };
constexpr int Q_WX[17] = { 20, 8,11,11,11,11,11,11,11,11,11,11,11,11,11,11,11 };
constexpr int town(int i) { return i; }
enum { IT_X, IT_W, IT_H, IT_C, IT_O, IT_S, IT_M, IT_P, IT_T, IT_D };
constexpr int cu_lds(int u) {
  using C = Cfg<true>;
  return u < 5 ? C::XSLOT + u * 4096 : u < 9 ? C::LW + 2 * C::WSLOT + (u - 5) * 4096 : u < 13 ? C::LW + 3 * C::WSLOT + (u - 9) * 4096
       : u < 15 ? L_EB + 16384 + (u - 13) * 4096 : u < 20 ? 2 * C::XSLOT + (u - 15) * 4096 : C::LW + (u - 20) * 4096;
}
constexpr int O_ORD[14] = {8, 9, 12, 13, 6, 7, 10, 11, 5, 4, 3, 2, 1, 0};
struct Sched {
  int n = 0; bool ok = true; int kind[192] = {}, idx[192] = {}, sub[192] = {}, after[192] = {}, need[192] = {};
  int hdrain[14] = {}, hlds[14] = {};
  constexpr void add(int k, int i, int p, int a, int nd) { kind[n] = k; idx[n] = i; sub[n] = p; after[n] = a; need[n] = nd; n++; if (nd <= a) ok = false; }
  constexpr int allowed(int b) const {
    int last = -1; for (int i = 0; i < n; i++) if (after[i] < b && need[i] <= b) last = i;
    int c = 0; for (int i = last + 1; i < n; i++) if (after[i] < b) c++;
    return c > 63 ? 63 : c;
  }
  constexpr bool needed(int b) const { for (int i = 0; i < n; i++) if (need[i] == b) return true; return false; }
  constexpr int between(int i0, int i1) const { return i1 - i0 - 1; }
  constexpr int pos(int it) const { int c = 0; for (int i = 0; i < it; i++) if (after[i] == after[it]) c++; return c; }
  constexpr int count(int b) const { int c = 0; for (int i = 0; i < n; i++) if (after[i] == b) c++; return c; }
  constexpr int drain_ord(int b, int f) const { if (b < 1 || hdrain[f / 2] != b) return -1; int c = 0; for (int i = 0; i < f; i++) if (hdrain[i / 2] == b) c++; return c; }
  constexpr bool chained(int i) const {
    return i > 0 && kind[i] <= IT_W && kind[i] == kind[i - 1] && idx[i] == idx[i - 1] && (sub[i] >> 2) == (sub[i - 1] >> 2); }
  constexpr int find(int k) const { for (int j = 0; j < n; j++) if (kind[j] == k) return j; return -1; }
};
template <bool FFN> constexpr Sched make_sched() {
  using C = Cfg<FFN>;
  Sched s;
  int wi = 0, xi = 0, hi = 0, ci = 0, oi = 0, hl = 0;
  int ebfree[4] = {-2, -2, -2, -2};
  for (int b = -2; b <= 14; b++) {
    if (b == 7) s.add(IT_P, 0, 0, b, 99);
    if (b == (FFN ? 7 : 5)) s.add(IT_S, 0, 0, b, 99);
    for (int cap = FFN ? F_WX[b + 2] : Q_WX[b + 2]; cap > 0; cap--) {
      const int wk = wi / C::WP, xs = xi / C::XP;
      const bool wok = wk < 14 && (wk < C::NWS || wk - C::NWS <= b), xok = xs < 7 && (xs < 3 || 2 * (xs - 2) <= b);
      if (wok && (!xok || wk - 1 <= 2 * xs - 1)) { s.add(IT_W, wk, wi % C::WP, b, FFN && wk == 13 ? 11 : wk - 1); wi++; }
      else if (xok) { s.add(IT_X, xs, xi % C::XP, b, 2 * xs - 1); xi++; }
    }
    if (b == (FFN ? 9 : 7)) s.add(IT_M, 0, 0, b, b + 2);
    if (FFN) {
      for (int k = 0; k < F_H[b + 2]; k++, hi++) {
        if (b < 13) { int sl = hi & 3; if (b >= 11) { sl = 3; while (sl > 0 && (sl == 2 || ebfree[sl] > b)) sl--; }
          if (ebfree[sl] > b) s.ok = false; s.hlds[hi] = L_EB + sl * 8192; s.hdrain[hi] = b == 12 ? 13 : b + 2; ebfree[sl] = s.hdrain[hi] + 1; }
        else { if (b == 14 && hl < 2) hl = 2; if (hl >= (b == 13 ? 2 : 4)) s.ok = false; s.hlds[hi] = (hl >> 1) * 61440 + (hl & 1) * 8192; s.hdrain[hi] = 15; hl++; }
        s.add(IT_H, hi, 0, b, s.hdrain[hi]); s.add(IT_H, hi, 1, b, s.hdrain[hi]);
      }
      for (int k = 0; k < F_C[b + 2]; k++, ci++) {
        if (b < (ci < 9 ? 10 : ci < 15 ? 11 : 12) || (ci == 13 && ebfree[2] > b)) s.ok = false;
        if (ci == 14) ebfree[2] = 99;
        s.add(IT_C, ci, 0, b, 13);
      }
      for (int k = 0; k < F_O[b + 2]; k++, oi++) {
        if (oi < 6) { const int sl = (O_ORD[oi] - 6) >> 1; if (ebfree[sl] > b && ebfree[sl] < 99) s.ok = false; ebfree[sl] = 99; } else if (b < 14) s.ok = false;
        s.add(IT_O, O_ORD[oi], 0, b, 99);
      }
    }
    if (FFN && b == 12) for (int k = 0; k < 6; k++) s.add(IT_D, 0, k, b, 99);
    if (b == -2 && town(0) != (FFN ? 2 : 1)) s.add(IT_T, 0, 0, b, 99);
    if (b == 0 && town(1) != (FFN ? 2 : 1)) s.add(IT_T, 1, 0, b, 99);
    if (b == 1 && town(2) != (FFN ? 2 : 1)) s.add(IT_T, 2, 0, b, 99);
  }
  if (wi != 14 * C::WP || xi != 7 * C::XP || (FFN && (hi != 14 || ci != 28 || oi != 14))) s.ok = false;
  return s;
}
static_assert(make_sched<true>().ok && make_sched<false>().ok, "schedule complete and free of LDS hazards");

struct Args { const u16 *x, *wqkv, *wo, *wg, *wu, *wd; u16* out; char* ws; };
template <int V> using IC = std::integral_constant<int, V>;

DEV u32 lane_id() { return __builtin_amdgcn_mbcnt_hi(~0u, __builtin_amdgcn_mbcnt_lo(~0u, 0u)); }
DEV u64 uni64(u64 v) { return (u64)(u32)__builtin_amdgcn_readfirstlane((u32)v) | ((u64)(u32)__builtin_amdgcn_readfirstlane((u32)(v >> 32)) << 32); }
DEV void poll64(const void* p, u64 tag) {
#pragma unroll 1
  for (u32 sp = 0; sp < SPIN_CAP; sp++) {
    u64 v; asm volatile("global_load_dwordx2 %0, %1, off sc1\n\ts_waitcnt vmcnt(0)" : "=v"(v) : "v"(p) : "memory");
    if (__all(v == tag)) break;
  }
}
DEV void settle(u64& v, const void* p, u32 tag) {
  if (!__all((u32)(v >> 32) == tag)) {
#pragma unroll 1
    for (u32 sp = 0; sp < SPIN_CAP; sp++) { async_ld64_sc1(v, p); wait_vm_for<0>(v); if (__all((u32)(v >> 32) == tag)) break; }
  }
}
DEV float half_sum(float v) {
  v += dppf<0x128>(v); v += dppf<0x124>(v); v += dppf<0x122>(v); v += dppf<0x121>(v);
  auto r = __builtin_amdgcn_permlane16_swap(__float_as_uint(v), __float_as_uint(v), false, false);
  return __uint_as_float(r[0]) + __uint_as_float(r[1]);
}

DEV void fmac(float& a, float b, float c) { asm volatile("v_fmac_f32 %0, %1, %2" : "+v"(a) : "v"(b), "v"(c)); }
template <bool FFN> constexpr int rbo(int i) { return !FFN ? i : i == 0 ? 4 : i <= 4 ? i - 1 : i; }
template <bool FFN, class Store> DEV void gemm1(f32x4 (&acc)[Cfg<FFN>::NRB][4], const u32 (&wa)[4], int lane, Store&& store) {
  using C = Cfg<FFN>;
  constexpr int NRB = C::NRB, NA = FFN ? 2 : 4;
  const int j = lane & 15, g = lane >> 4;
  const u32 xdl = (j == 15) ? (NRB - C::ONES_RB) * 2048 - 14 * 128 : 0;
  u32 xa[2], wb[4];
  xa[0] = opq(j * 128 + ((g ^ (j >> 1)) << 4) + C::LX); xa[1] = 0;
  for (int i = 0; i < NA; i++) wb[i] = opq(wa[i] + C::LW);
  auto ldx = [&](auto hc, auto rc) INL { constexpr int R = rbo<FFN>(rc); return lds128((R == C::ONES_RB ? xa[hc] + xdl : xa[hc]) + R * 2048); };
  auto wad = [&](auto oc, auto fc) INL { constexpr int Fi = fc; return (FFN ? wb[Fi & 1] + (Fi >> 1) * 8192 : wb[Fi]) + oc; };
  bf16x8 xb[4]; s16x4 wl[2][4], wh[2][4];
  wait_lgkm0();
  bar();
  sfor<4>([&](auto f) INL { wl[0][f] = ldtr(wad(IC<0>{}, f)); SBAR(); wh[0][f] = ldtr(wad(IC<0>{}, f) + 1024); SBAR(); if constexpr (f > 0) xb[f - 1] = ldx(IC<0>{}, IC<f - 1>{}); SBAR(); });
  int xs = 0, wsl = 0, dx = 0, dw = 0;
  asm volatile(".p2align 5\n\t.rept %0\n\ts_nop 0\n\t.endr" :: "i"(FFN ? 2 : 4));
#pragma unroll 1
  for (int it = 0; it < (FFN ? 6 : 7);) {
    sfor<2>([&](auto hc) INL { constexpr int H = hc;
      sfor<NRB>([&](auto rc) INL { constexpr int R = rc;
        auto M = [&](auto f) INL {
          mfma_acc(acc[rbo<FFN>(R)][f], __builtin_shufflevector(wl[H][f], wh[H][f], 0, 1, 2, 3, 4, 5, 6, 7), xb[(H * NRB + R) & 3]); SBAR(); };
        constexpr bool PW = R >= NRB - 4;
        constexpr int WO = (FFN && H == 0) ? C::WSLOT : 0;
        M(IC<0>{});
        if constexpr (R == NRB - 4) bar();
        if constexpr (PW) wl[H ^ 1][R - (NRB - 4)] = ldtr(wad(IC<WO>{}, IC<R - (NRB - 4)>{}));
        if constexpr (H == 0 && R == 1) xa[1] = opq(xa[0] ^ 64);
        if constexpr (H == 0 && R == 2) { dx = xs == 2 ? -2 * C::XSLOT : C::XSLOT; pins(dx); }
        if constexpr (H == 0 && R == 3) { xs = xs == 2 ? -1 : xs; pins(xs); }
        if constexpr (H == 1 && R == 1) { xa[0] = opq(xa[0] + dx); xs++; pins(xs); }
        if constexpr (FFN && H == 0 && R == 4) { dw = (it & 1) ? -2 * C::WSLOT : 2 * C::WSLOT; pins(dw); }
        if constexpr (!FFN && R == 0) { dw = wsl == 2 ? -2 * C::WSLOT : C::WSLOT; pins(dw); }
        if constexpr (FFN && H == 1 && (R == 2 || R == 3)) wb[R - 2] = opq(wb[R - 2] + dw);
        if constexpr (!FFN && R >= 4 && R < 8) wb[R - 4] = opq(wb[R - 4] + dw);
        if constexpr (H == 1 && R == 5) { it++; pins(it); }
        SBAR();
        M(IC<1>{});
        if constexpr (!FFN && R == 1) { wsl = wsl == 2 ? -1 : wsl; pins(wsl); }
        if constexpr (!FFN && R == 2) { wsl++; pins(wsl); }
        if constexpr (PW) wh[H ^ 1][R - (NRB - 4)] = ldtr(wad(IC<WO + 1024>{}, IC<R - (NRB - 4)>{}));
        SBAR();
        M(IC<2>{});
        if constexpr (R + 3 < NRB) xb[(H * NRB + R + 3) & 3] = ldx(hc, IC<R + 3>{});
        else xb[(H * NRB + R + 3) & 3] = ldx(IC<(H ^ 1)>{}, IC<R + 3 - NRB>{});
        SBAR();
        M(IC<3>{});
      });
    });
  }
  if constexpr (FFN) {
    auto gh = [](int G) constexpr { return G < 3 ? 0 : G >= 17 ? 1 : (G & 1); };
    auto gi = [](int G) constexpr { return G < 3 ? G : G >= 17 ? G - 10 : (G & 1) ? (G - 3) / 2 : (G + 2) / 2; };
    constexpr int FO[4] = {0, 2, 1, 3};
    u32x4 st[3], o[2]; float cs[16], nm[2];
    xa[1] = opq(xa[0] ^ 64);
    const u32 sta = opq(L_STAT + j * 48);
    auto epi = [&](auto ic, auto kc) INL { constexpr int I = ic, k = kc, R = rbo<true>(I), hf = (k - 1) / 14, q = (k - 1) % 14;
      if constexpr (k == 0) { nm[I & 1] = bflo(st[R >> 2][R & 3]); asm volatile("" : "+v"(nm[I & 1])); }
      else if constexpr (k == 29) store(IC<R>{}, o[I & 1]);
      else if constexpr (q < 8) { float t = acc[R][hf + 2 * (q >> 2)][q & 3]; fmac(t, nm[I & 1], cs[(hf + 2 * (q >> 2)) * 4 + (q & 3)]); acc[R][hf + 2 * (q >> 2)][q & 3] = t; }
      else if constexpr (q < 12) { float t = acc[R][hf][q - 8] * acc[R][hf + 2][q - 8]; asm volatile("" : "+v"(t)); acc[R][hf][q - 8] = t; }
      else { u32 t = pk(acc[R][hf][2 * (q - 12)], acc[R][hf][2 * (q - 12) + 1]); asm volatile("" : "+v"(t)); o[I & 1][2 * hf + q - 12] = t; } };
    auto filler = [&](auto nc) INL { constexpr int n = nc;
      if constexpr (n < 16) { constexpr int f = FO[n >> 2]; cs[f * 4 + (n & 3)] = dppf<0x15f>(acc[4][f][n & 3]); asm volatile("" : "+v"(cs[f * 4 + (n & 3)])); }
      else epi(IC<(n - 16) / 30>{}, IC<(n - 16) % 30>{}); };
    sfor<20>([&](auto gc) INL { constexpr int G = gc, H = gh(G), R = rbo<true>(gi(G));
      sfor<4>([&](auto sc) INL { constexpr int q = sc, f = FO[q], gap = G * 4 + q;
        mfma_acc(acc[R][f], __builtin_shufflevector(wl[H][f], wh[H][f], 0, 1, 2, 3, 4, 5, 6, 7), xb[G & 3]); SBAR();
        if constexpr (G < 3 && q != 2 && G * 3 + q - (q > 2) < 8) { constexpr int t = G * 3 + q - (q > 2), wf = FO[t >> 1];
          if constexpr (t & 1) wh[1][wf] = ldtr(wad(IC<C::WSLOT + 1024>{}, IC<wf>{})); else wl[1][wf] = ldtr(wad(IC<C::WSLOT>{}, IC<wf>{})); }
        if constexpr (q == 2 && G + 3 < 20) xb[(G + 3) & 3] = ldx(IC<gh(G + 3)>{}, IC<gi(G + 3)>{});
        if constexpr (G == 3 && q < 2) st[q] = *(const LAS u32x4*)(sta + q * 16);
        if constexpr (G == 3 && q == 3) { const u32x2 t = *(const LAS u32x2*)(sta + 32); st[2][0] = t[0]; st[2][1] = t[1]; }
        if constexpr (G == 4 && q == 0) { bar(); }
        if constexpr (gap >= 17) sfor<4>([&](auto e) INL { filler(IC<(gap - 17) * 4 + e>{}); });
        SBAR();
      });
    });
    sfor<16 + 8 * 30 - 63 * 4>([&](auto e) INL { filler(IC<63 * 4 + e>{}); });
    mfma_fence();
    sfor<2>([&](auto ic) INL { constexpr int R = 8 + ic;
      const float m = bflo(st[R >> 2][R & 3]);
      f32x4 c[4]; for (int f = 0; f < 4; f++) c[f] = f32x4{cs[4 * f], cs[4 * f + 1], cs[4 * f + 2], cs[4 * f + 3]};
      const f32x4 h0 = (acc[R][0] + m * c[0]) * (acc[R][2] + m * c[2]), h1 = (acc[R][1] + m * c[1]) * (acc[R][3] + m * c[3]);
      store(IC<R>{}, u32x4{pk(h0[0], h0[1]), pk(h0[2], h0[3]), pk(h1[0], h1[1]), pk(h1[2], h1[3])});
      SBAR(); asm volatile("s_nop 1"); SBAR(); });
  } else mfma_fence();
}

DEV u32 w2_lane_addr(int lane, int cb, int row0) {
  const int row = row0 + (lane >> 2);
  return row * 128 + ((cb ^ ((row >> 1) & 3)) << 5) + (lane & 3) * 8;
}

constexpr int rslot(int r) { constexpr int o[5] = {1, 2, 4, 0, 3}; return o[r % 5] * RSLOT; }
constexpr int L_FX = L_WO;
template <bool CA> struct Side {
  static constexpr int NO = CA ? 3 : 2, RB0 = CA ? 0 : 3;
  u32x2 xr[NO]; u64 gr[NO];
  static DEV const char* gp(const char* ws, int rt, int i, int lane) { return ws + WS_STAT + min(rt * T2ROWS + (RB0 + i) * 16 + (lane & 15), S_ - 1) * 8; }
  static DEV u32 out_off(int rt, int ct, int cb, int i, int ln) {
    const u32 o = (rt * T2ROWS + (RB0 + i) * 16 + (ln >> 2)) * XROWB + ct * 128 + cb * 32 + (ln & 3) * 8;
    return (RB0 + i == 4 && ln >= 60) ? 0x80000000u : o; }
  const char* ga[NO]; u32 oo[NO];
  DEV void prepare(const Args& A, int wv, int lane, int rt, int ct) {
    for (int i = 0; i < NO; i++) { ga[i] = gp(A.ws, rt, i, lane); pinp(ga[i]); oo[i] = opq(out_off(rt, ct, wv, i, lane)); } }
  DEV void request(const Args& A) {
    const srd_t sx = make_srd(A.x, S_ * XROWB);
    for (int i = 0; i < NO; i++) { async_ld64_sc1(gr[i], ga[i]); async_bld64(xr[i], oo[i], sx); }
  }
};
template <bool CA>
DEV void stage2(const Args& A, Side<CA>& sd, bf16x8 (&wd)[14][2], int wv, int lane, int rt, int ct, u64 tag) {
  char* ws = A.ws;
  constexpr int NO = CA ? 3 : 2, RB0 = CA ? 0 : 3;
  const srd_t sact = make_srd(ws + WS_ACT + (size_t)rt * ACT_TILE, ACT_TILE);
  const u32 vlin = lane * 16;
  const u32 lpart = wv * 5120;
  auto issue = [&](auto rc, auto&& fill) INL { constexpr int R = rc, SI = (R < 7 ? 7 + R : R < 14 ? R - 7 : R) * RSLOT;
    const u32 so = dma_set<rslot(R), SI>(lpart, lpart);
    sfor<4>([&](auto p) INL { dma_sc1<p * 1024>(vlin, sact, so); fill(p); SBAR(); });
    const u32 s4 = dma_set<rslot(R) + 4096, SI + 4096>(lpart, lpart);
    dma_sc1<0>(vlin, sact, s4); fill(IC<4>{}); SBAR(); };
  auto nofill = [](auto) INL {};
  auto stream = [&](auto r0c, auto nrc, auto nbc, auto rbc, auto dic, auto& wf, auto& ac, auto&& at_barrier, auto&& fill) INL {
    constexpr int R0 = r0c, NB = nbc, RB = rbc, DI = dic, PR = 4 * NB, NT = nrc * PR;
    bf16x8 fr[DI + 1]; u32 a[2];
    auto base = [&](auto rc) INL { a[rc & 1] = opq(vlin + rslot(R0 + rc)); };
    auto rd = [&](auto tc) INL { constexpr int T = tc, I = T % PR; fr[T % (DI + 1)] = lds128(a[(T / PR) & 1] + (I / NB) * 5120 + (RB + I % NB) * 1024); };
    base(IC<0>{});
    sfor<DI>([&](auto t) INL { rd(t); });
    SBAR();
    sfor<NT>([&](auto tc) INL { constexpr int T = tc, R = T / PR, I = T % PR, U = T / NB;
      if constexpr (U == 0) mfma_new(ac[I % NB], wf[U >> 1][U & 1], fr[T % (DI + 1)]); else mfma_acc(ac[I % NB], wf[U >> 1][U & 1], fr[T % (DI + 1)]);
      SBAR();
      if constexpr (I == PR - DI && R0 + R < 17) { at_barrier(IC<R0 + R + 1>{}); bar(); }
      if constexpr (I == PR - DI - 2 && T + PR < NT) base(IC<R + 1>{});
      if constexpr (T + DI < NT) rd(IC<T + DI>{});
      fill(tc);
      SBAR();
    }); };
  float c1[NO], d2[NO];
  u32x2 (&xr)[NO] = sd.xr; u64 (&gr)[NO] = sd.gr;
  auto side_done = [&]() INL {
    const int dst = (((lane & 15) << 2) | (lane >> 4)) << 2;
    sfor<NO>([&](auto i) INL { asm volatile("" : "+v"(gr[i])); asm volatile("" : "+v"(xr[i]));
      xr[i][0] = __builtin_amdgcn_ds_bpermute(dst, xr[i][0]); xr[i][1] = __builtin_amdgcn_ds_bpermute(dst, xr[i][1]);
      settle(gr[i], sd.ga[i], (u32)tag);
      const float d = bfhi((u32)gr[i]); d2[i] = d * d; c1[i] = __builtin_amdgcn_rcpf(d2[i]) * -1.4426950408889634f;
      asm volatile("" : "+v"(c1[i]), "+v"(d2[i]), "+v"(xr[i])); }); };
  bf16x8 wo_f[8][2];
  u32 woa[2][2];
  for (int e = 0; e < 2; e++) { woa[e][0] = opq(L_WO + w2_lane_addr(lane, wv, 28 * e)); woa[e][1] = opq(L_WO + w2_lane_addr(lane, wv, 28 * e + (lane >= 48 ? 12 : 16))); }
  auto read_wo = [&](auto i) INL { for (int e = 0; e < 2; e++) wo_f[i][e] = ldtr2(woa[e][0] + i * 56 * 128, woa[e][1] + i * 56 * 128); };
  f32x4 af[5], as[NO];
  if constexpr (CA) {
    static constexpr Sched SC = make_sched<true>();
    u32 dl[2]; dl[0] = opq(w2_lane_addr(lane, wv, 0)); dl[1] = opq(dl[0] + 61440);
    wait_vm<0>();
    side_done();
    bar();
    sfor<28>([&](auto fc) INL { constexpr int e = SC.hlds[fc / 2] >= 61440, o = SC.hlds[fc / 2] - e * 61440 + (fc & 1) * 4096;
      if constexpr (SC.hdrain[fc / 2] == 15) wd[fc / 2][fc & 1] = ldtr2(dl[e] + o, dl[e] + o + 2048); });
    sfor<8>(read_wo);
    wait_lgkm0(); bar();
    sfor<7>([&](auto n) INL { issue(IC<3 + n>{}, nofill); if constexpr (n >= 2) wait_vm<10>(); bar(); });
    stream(IC<7>{}, IC<7>{}, IC<5>{}, IC<0>{}, IC<7>{}, wd, af, [&](auto nc) INL { constexpr int N = nc; if constexpr (N == 8) wait_vm<5>(); if constexpr (N == 9) wait_vm<0>(); }, nofill);
    __builtin_amdgcn_s_setprio(1);
  } else {
    const char* ofp = ws + WS_OFLAG + ((((rt * 5 + (wv >> 1) * 4) * 187) >> 11) * 6 + (wv & 1) * 3 + min(lane >> 2, 2)) * FSTR;
    asm volatile("" : "+v"(ofp));
    side_done();
    __builtin_amdgcn_s_sleep(12);
    poll64(ws + WS_HFLAG + ((rt >> 1) * 14 + min(lane >> 2, 13)) * FSTR + (lane & 3) * 8, tag);
    issue(IC<0>{}, nofill);
    bar();
    sfor<2>([&](auto n) INL { issue(IC<1 + n>{}, [&](auto p) INL { if constexpr (p < 4) read_wo(IC<4 * n + p>{}); }); });
    wait_vm<10>(); bar();
    stream(IC<0>{}, IC<7>{}, IC<5>{}, IC<0>{}, IC<7>{}, wd, af, [&](auto nc) INL { constexpr int N = nc; if constexpr (N == 1) wait_vm<5>(); if constexpr (N == 2) wait_vm<0>(); }, nofill);
    u64 of;
    sfor<7>([&](auto n) INL {
      if constexpr (n == 2) async_ld64_sc1(of, ofp);
      issue(IC<10 + n>{}, nofill);
      if constexpr (n == 3) { wait_vm_for<10>(of); if (!__all(of == tag)) poll64(ofp, tag); }
      else if constexpr (n == 2) wait_vm<11>();
      else if constexpr (n > 2) wait_vm<10>();
      bar(); });
    issue(IC<17>{}, nofill);
  }
  f32x4 pf[NO], xf[NO]; u32 (&oo)[NO] = sd.oo; u32 fx; int src;
  constexpr int DS = 4, T17 = 12 * NO - DS;
  stream(IC<14>{}, IC<4>{}, IC<NO>{}, IC<RB0>{}, IC<DS>{}, wo_f, as,
         [&](auto nc) INL { constexpr int N = nc; if constexpr (!CA) wait_vm<5 * (17 - N)>(); },
         [&](auto tc) INL { constexpr int T = tc, k = T - 2;
           if constexpr (k >= 0 && k < 2 * NO) { constexpr int i = k >> 1, e = k & 1;
             xf[i][2 * e] = bflo(xr[i][e]); xf[i][2 * e + 1] = bfhi(xr[i][e]); asm volatile("" : "+v"(xf[i][2 * e]), "+v"(xf[i][2 * e + 1])); }
           else if constexpr (k == 3 * NO) src = opq(((lane >> 2) | ((lane & 3) << 4)) << 2);
           else if constexpr (k == 3 * NO + 1) fx = opq(L_FX + wv * 1024 + vlin);
           if constexpr (T >= T17 && T < T17 + 5 - NO) { constexpr int rb = (CA ? 3 : 0) + T - T17; *(LAS f32x4*)(fx + rb * 4096) = af[rb]; }
           if constexpr (T == T17 + 5 - NO + DS) { bar(); sfor<NO>([&](auto i) INL { pf[i] = lds128f(fx + (RB0 + i) * 4096); }); } });
  static_assert(T17 + 5 - NO + DS < 16 * NO, "M1 inside the stream");
  f32x4 gt[NO];
  sfor<NO>([&](auto i) INL {
    const f32x4 f = af[RB0 + i] + pf[i], t = f * c1[i];
    f32x4 e; for (int r = 0; r < 4; r++) e[r] = __builtin_amdgcn_exp2f(t[r]);
    e = e * d2[i] + d2[i];
    for (int r = 0; r < 4; r++) e[r] = __builtin_amdgcn_rcpf(e[r]);
    gt[i] = f * e;
    if constexpr (i == 0) { asm volatile("" : "+v"(gt[0])); SBAR(); }
    const f32x4 v = as[i] * gt[i] + xf[i];
    xr[i][0] = __builtin_amdgcn_ds_bpermute(src, pk(v[0], v[1])); xr[i][1] = __builtin_amdgcn_ds_bpermute(src, pk(v[2], v[3])); SBAR(); });
  const __amdgpu_buffer_rsrc_t ro = rsrc(A.out, S_ * XROWB);
  sfor<NO>([&](auto i) INL { __builtin_amdgcn_raw_buffer_store_b64(xr[i], ro, oo[i], 0, 0); });
}

template <bool FFN, class Hook>
DEV void loader(Hook&& hook, const Args& A, bf16x8 (&wdf)[14][2], int hw, int lane, int tile, int grp, int ct, u32 bid_, u32 rid, u32 code_off, u64 code_base) {
  using C = Cfg<FFN>;
  static constexpr Sched SC = make_sched<FFN>();
  constexpr int WROWB = FFN ? GROWB : QROWB;
  const int row0 = FFN ? tile * (2 * T2ROWS) : blk_row(tile * 11) - 14;
  auto grow = [&](int L) INL { return max(0, min(row0 + L - (FFN && L >= 80 ? 1 : 0), S_ - 1)); };
  u32 wv[C::WP], wsb, wlb;
  {
    const u32 w0 = (lane >> 4) * WROWB + (((lane & 15) ^ ((lane >> 4) << 1)) << 4);
    if constexpr (FFN) {
      for (int p = 0; p < 4; p++) wv[p] = (w0 ^ ((p >> 1) << 7)) + p * (4 * WROWB - 1024);
      wsb = (hw >> 1) * 16 * WROWB + grp * 256; wlb = (hw & 1) * 8192 + (hw >> 1) * 4096;
    } else {
      const u32 w1 = (lane >> 4) * WROWB + (((lane & 15) ^ (((lane >> 4) | ((hw & 1) << 2)) << 1)) << 4);
      for (int p = 0; p < 6; p++) wv[p] = w1 + (p & 1) * 4 * WROWB + (p >> 1) * XROWB + 4096 - (p & 3) * 1024;
      wsb = hw * 8 * WROWB + grp * 128; wlb = hw * 6144;
    }
  }
  u32 xv[C::XP];
  auto x_offsets = [&]() INL {
    const int ln = opq(lane), rb = row0 + hw * C::XP * 8 - (FFN ? hw >> 1 : 0) + (ln >> 3);
    const u32 sw = ((ln & 7) ^ (ln >> 4) ^ (FFN ? (hw & 1) << 2 : 0)) << 4;
    for (int i = 0; i < C::XP; i++) xv[i] = opq((FFN || i >= 2 ? rb + 8 * i : max(rb + 8 * i, 0)) * XROWB + (sw ^ ((i & 1) << 6)) + 4096 - (i & 3) * 1024); };
  for (int p = 0; p < C::WP; p++) wv[p] = opq(wv[p]);
  SBAR();
  const srd_t sw = FFN ? make_srd((hw & 1) ? A.wu : A.wg, D_ * GROWB) : make_srd((const char*)A.wqkv - 4096, D_ * QROWB + 4096);
  const srd_t sx = make_srd((const char*)A.x - 4096, S_ * XROWB + 4096);
  const srd_t sd = make_srd(A.wd, F_ * XROWB), so = make_srd(A.wo, D_ * XROWB);
  const u32 xlb = hw * C::XP * 1024;
  u32 dv;
  const u32 dsb = hw * 8 * XROWB + ct * 128, dlb = hw * 1024;
  const u32 zero = 0;
  u64 gran = 0;
  u32 cur = 0;
  int srow; const char* gp;
  u64 tagl = 0;
  u32 da;
  auto drain = [&](auto bc, auto lo, auto hi) INL {
    sfor<28>([&](auto fc) INL { constexpr int H = fc / 2, c = fc & 1, ord = SC.drain_ord(bc, fc);
      if constexpr (FFN && ord >= lo && ord < hi) { constexpr int o = SC.hlds[H] - L_EB + c * 4096; static_assert(o >= 0 && o < 32768, "bounce buffer");
        wdf[H][c] = ldtr2(da + o, da + o + 2048); SBAR(); } }); };
  const void* tp[3]; u32 to[3], co;
  auto t_prep = [&](auto idc) INL { constexpr int id = idc, NW = town(id) == 1 ? 182 : town(id) == 2 ? 72 : 256;
    const u32 bid = town(id) ? rid : bid_, ln = opq(lane);
    auto lines = [&](u32 first, u32 n, u32 end) INL { return opq(min(first + bid * n + min(ln, n - 1), end - 1) * 128); };
    if constexpr (id == 2) { constexpr int n = (F_ * 7 / 4 + NW - 1) / NW; tp[2] = A.wd; to[2] = lines(hw * n * NW, n, F_ * 7); }
    else {
      constexpr int K0 = id ? WK1 : WK0, K1 = id ? D_ : WK1, no = (D_ * 7 / 2 + NW - 1) / NW;
      static_assert((K1 - K0) * 28 <= NW * 64, "one line per lane");
      const u32 lpr = hw == 2 ? 21 : 28;
      tp[id] = karg((0x2143 >> (4 * hw)) & 7);
      to[id] = lines(hw == 3 ? id * (D_ * 7 / 2) : K0 * lpr, hw == 3 ? no : hw == 2 ? ((K1 - K0) * 21 + NW - 1) / NW : ((K1 - K0) * 28 + NW - 1) / NW, hw == 3 ? D_ * 7 : K1 * lpr);
    } };
  auto issue_after = [&](auto bc) INL { constexpr int B = bc;
    sfor<SC.n>([&](auto ic) INL { constexpr int I = ic;
      if constexpr (SC.after[I] == B) {
        constexpr int id = SC.idx[I], p = SC.sub[I], pos = SC.pos(I);
        if constexpr (I == 2) x_offsets();
        if constexpr (pos >= 2) drain(bc, IC<pos - 2>{}, IC<pos - 1>{});
        if constexpr (FFN && B == 12 && pos > 0 && SC.kind[I] != IT_D) __builtin_amdgcn_s_sleep(1);
        if constexpr (SC.kind[I] == IT_X) {
          if constexpr (!SC.chained(I)) cur = dma_set<C::LX + (id % 3) * C::XSLOT + (p >> 2) * 4096, id * 128>(zero, xlb);
          dma<(p & 3) * 1024>(xv[p], sx, cur); }
        else if constexpr (SC.kind[I] == IT_W) {
          if constexpr (!SC.chained(I)) cur = dma_set<C::LW + (id % C::NWS) * C::WSLOT + (p >> 2) * 4096, id * 32 * WROWB>(wsb, wlb);
          dma<(p & 3) * 1024>(wv[p], sw, cur); }
        else if constexpr (SC.kind[I] == IT_H) dma<0>(dv, sd, dma_set<SC.hlds[id] + p * 4096, (2 * id + p) * 32 * XROWB>(dsb, dlb));
        else if constexpr (SC.kind[I] == IT_C) dma<0>(dv, sd, dma_set<cu_lds(id), (28 + id) * 32 * XROWB>(dsb, dlb));
        else if constexpr (SC.kind[I] == IT_O) dma<0>(dv, so, dma_set<L_WO + id * 4096, id * 32 * XROWB>(dsb, dlb));
        else if constexpr (SC.kind[I] == IT_T) { if constexpr (B == -2) t_prep(IC<id>{}); touch_at(tp[id], to[id]); }
        else if constexpr (SC.kind[I] == IT_D) { if constexpr (p == 0) hook(IC<0>{}); }
        else if constexpr (SC.kind[I] == IT_P) touch_at((const void*)code_base, co);
        else if constexpr (SC.kind[I] == IT_M) {
          wait_vm_for<SC.between(SC.find(IT_S), I)>(gran);
          settle(gran, gp, (u32)tagl);
          if (lane < 48) dma_word(gp, (FFN ? L_STAT : L_DENW) + hw * 192); }
        else async_ld64_sc1(gran, gp);
      } });
    drain(bc, IC<(SC.count(B) >= 2 ? SC.count(B) - 2 : 0)>{}, IC<99>{}); };
  issue_after(IC<-2>{});
  if constexpr (FFN) { const int ln = opq(lane); da = opq(w2_lane_addr(ln, hw, 0) + L_EB); dv = opq((ln >> 3) * XROWB + (((ln & 7) ^ ((ln >> 4) << 1)) << 4)); }
  {
    sfor<SC.n>([&](auto ic) INL { if constexpr (SC.kind[ic] == IT_T && SC.after[ic] > -2) t_prep(IC<SC.idx[ic]>{}); });
    const int ln = opq(lane), sd_ = hw * 48 + min(ln, 47), sj = (sd_ * 171) >> 11;
    srow = FFN ? min(sd_ - 12 * sj, 9) * 16 + sj : sd_; gp = A.ws + WS_STAT + grow(srow) * 8; pinp(gp);
    if constexpr (!FFN) srow = opq(L_STAT + (srow & 15) * 48 + (srow >> 4) * 4);
    co = opq(code_off + (ln >> 3) * 128);
    if constexpr (FFN) hook(IC<1>{}); else { hook(IC<0>{}); hook(IC<1>{}); }
  }
  constexpr int LASTB = 14;
  sfor<LASTB + 2>([&](auto bc) INL { constexpr int B = bc - 1;
    if constexpr (B == 0) tagl = uni64(*(volatile LAS u64*)L_TAG);
    if constexpr (SC.needed(B)) wait_vm<SC.allowed(B)>();
    wait_lgkm0();
    bar();
    issue_after(IC<B>{});
    if constexpr (!FFN && B == 12) {
      *(LAS float*)srow = bflo((u32)gran);
    }
    if constexpr (!FFN && B == 9) hook(IC<4>{});
    if constexpr (!FFN && B == 10) hook(IC<3>{});
    if constexpr (!FFN && B == 12) hook(IC<5>{});
    if constexpr (!FFN && B == 11) hook(IC<2>{});
    SBAR();
  });
}

constexpr int L_REC = 123904;
struct Blk { u32x4 a; f32x4 rl, rk; u32x4 den; };
struct AttSetup { Blk b0, b1, c; f32x4 bias, bias1; u32 me, wk, wq; };
template <int PART> DEV void att_setup(AttSetup& S, int hw, int tile, int lane) {
  const int j = lane & 15, g = lane >> 4, par = j & 1;
  if constexpr (PART == 0) {
    const u32 qrow = (14 + j) * IMGROW + g * 16, zr = L_ZERO + g * 16;
    S.b0.a = u32x4{par ? zr : qrow, par ? qrow : zr, (u32)(2 * j * IMGROW + 192 + g * 16), (u32)(2 * (lane >> 2) * IMGROW + 384 + (lane & 3) * 8)};
    S.me = par ? 0u : 0xffffffffu; S.wk = L_DENW + 32 * g; S.wq = L_DENW + (14 + j) * 4;
    asm volatile("" : "+v"(S.b0.a), "+v"(S.me), "+v"(S.wk), "+v"(S.wq));
  } else if constexpr (PART == 1) {
    const int lo = (j >> 1) - 4 * g, lo0 = (tile + hw == 0 ? 7 : 0) - 4 * g;
    for (int k = 0; k < 4; k++) { S.bias1[k] = (k >= lo && k <= lo + 7) ? 0.f : -__builtin_inff(); S.bias[k] = k >= lo0 ? S.bias1[k] : -__builtin_inff(); }
    asm volatile("" : "+v"(S.bias), "+v"(S.bias1));
  } else {
    const u32x4 a0 = S.b0.a; const u32 me = S.me;
    auto blk = [&](Blk& B, int i) INL {
      const u32 o = blk_row(tile * 11 + i) - blk_row(tile * 11), d = o * IMGROW;
      B.a = a0 + u32x4{me & d, ~me & d, d, d};
      u32 w[8]; const u32 wa = S.wk + o * 4;
      for (int k = 0; k < 8; k++) w[k] = *(const LAS u32*)(wa + k * 4);
      const float cq = __builtin_amdgcn_rcpf(bfhi(*(const LAS u32*)(S.wq + o * 4))) * (0.18898223650461362f * 1.4426950408889634f);
      for (int k = 0; k < 4; k++) { B.den[k] = __builtin_amdgcn_perm(w[4 * (k & 1) + 2 + (k >> 1)], w[4 * (k & 1) + (k >> 1)], 0x07060302u);
        B.rk[k] = __builtin_amdgcn_rcpf(bfhi((w[2 * k] & me) | (w[2 * k + 1] & ~me))); }
      for (int k = 0; k < 4; k++) { float t = B.rk[k] * cq; asm volatile("" : "+v"(t)); B.rl[k] = t; } };
    auto pin = [](Blk& B) INL { asm volatile("" : "+v"(B.a), "+v"(B.rl), "+v"(B.rk), "+v"(B.den)); };
    if constexpr (PART == 2) { blk(S.b0, hw); pin(S.b0); }
    if constexpr (PART == 3) { blk(S.b1, 4 + hw); pin(S.b1); }
    if constexpr (PART == 4) { blk(S.c, 7 + hw); pin(S.c); }
    if constexpr (PART == 5) {
      const u32 ra = L_REC + hw * 4096 + lane * 16;
      *(LAS u32x4*)ra = S.c.a; *(LAS f32x4*)(ra + 1024) = S.c.rl; *(LAS f32x4*)(ra + 2048) = S.c.rk; *(LAS u32x4*)(ra + 3072) = S.c.den;
      if (hw == 0) *(LAS f32x4*)(L_REC + 16384 + lane * 16) = S.bias1;
    }
  }
}
DEV void attention(const Args& A, AttSetup& S, int wid, int tile, int h0, int hh0, int lane) {
  const u32 me = (lane & 1) ? 0u : 0xffffffffu;
  const __amdgpu_buffer_rsrc_t ra = rsrc(A.ws + WS_ACT, 26 * ACT_TILE);
  const f32x4 z = {0.f, 0.f, 0.f, 0.f};
#pragma unroll 1
  for (int pass = wid >= 4 ? 0 : 1; pass < (wid == 7 ? 1 : 2); pass++) {
    const int B = tile * 11 + (wid >= 4 ? wid - 4 + 4 * pass : 7 + wid), B5 = (B * 205) >> 10;
    const u32 off = opq(B5 * (ACT_TILE - 5120) + B * 1024 + (56 + h0) * 5120 + lane * 16);
    const u32 off0 = opq(hh0 ? 0x80000000u : off);
    const f32x4 rk = S.b0.rk, rl = S.b0.rl, bias = S.bias;
    const bf16x8 den = __builtin_bit_cast(bf16x8, S.b0.den);
    const u32 qa0 = S.b0.a[0], qa1 = S.b0.a[1], ka = S.b0.a[2], va = S.b0.a[3];
    if (pass == 0 || wid < 4) { wait_lgkm0(); bar(); }
    {
      bf16x8 q0[3], q1[3], k0[3], k1[3], v0[3], v1[3];
      sfor<3>([&](auto h) INL { q0[h] = lds128(qa0 + h * 64); k0[h] = lds128(ka + h * 64); q1[h] = lds128(qa1 + h * 64); k1[h] = lds128(ka + IMGROW + h * 64); SBAR(); });
      f32x4 s[3];
      sfor<3>([&](auto h) INL { s[h] = mfma(k1[h], q1[h], mfma(k0[h], q0[h], bias));
        v0[h] = ldtr2(va + h * 64, va + IMGROW + h * 64); v1[h] = ldtr2(va + h * 64 + 32, va + IMGROW + h * 64 + 32); });
      SBAR();
      float m[3];
      sfor<3>([&](auto h) INL { s[h] *= rl; m[h] = fmaxf(fmaxf(fmaxf(s[h][0], s[h][1]), s[h][2]), s[h][3]); });
      SBAR();
      sfor<3>([&](auto h) INL { auto r = __builtin_amdgcn_permlane16_swap(__float_as_uint(m[h]), __float_as_uint(m[h]), false, false);
        m[h] = fmaxf(__uint_as_float(r[0]), __uint_as_float(r[1])); });
      SBAR();
      sfor<3>([&](auto h) INL { auto r = __builtin_amdgcn_permlane32_swap(__float_as_uint(m[h]), __float_as_uint(m[h]), false, false);
        m[h] = fmaxf(__uint_as_float(r[0]), __uint_as_float(r[1])); });
      SBAR();
      f32x4 sm[3], o0[3], o1[3];
      sfor<3>([&](auto h) INL {
        f32x4 e = s[h] - m[h];
        for (int r = 0; r < 4; r++) e[r] = __builtin_amdgcn_exp2f(e[r]);
        e *= rk;
        u32 p0 = pk(e[0], e[1]), p1 = pk(e[2], e[3]);
        asm volatile("" : "+v"(p0), "+v"(p1));
        s[h] = __builtin_bit_cast(f32x4, u32x4{p0 & me, p1 & me, p0 & ~me, p1 & ~me}); });
      SBAR();
      sfor<3>([&](auto h) INL { const bf16x8 pf = __builtin_bit_cast(bf16x8, s[h]); sm[h] = mfma(den, pf, z); o0[h] = mfma(v0[h], pf, z); o1[h] = mfma(v1[h], pf, z); });
      SBAR();
      sfor<3>([&](auto h) INL {
        const float rs = __builtin_amdgcn_rcpf(sm[h][0]);
        const f32x4 a = o0[h] * rs, c = o1[h] * rs;
        const u32x4 ov = {pk(a[0], a[1]), pk(a[2], a[3]), pk(c[0], c[1]), pk(c[2], c[3])};
        __builtin_amdgcn_raw_buffer_store_b128(ov, ra, h == 0 ? off0 : off, h * 5120, 16);
        SBAR(); });
    }
    if (pass == 0) bar();
    S.b0 = S.b1; S.bias = S.bias1;
  }
}

template <bool FFN> DEV u64 compute_head(const Args& A, f32x4 (&acc)[Cfg<FFN>::NRB][4], int bid, int cw, int lane) {
  const bool act = (lane & 31) < 28;
  const int srow = bid * 8 + cw * 2 + (lane >> 5);
  char* ws = A.ws;
  if (lane < 16) *(LAS u32x4*)((cw < 3 ? Cfg<FFN>::XSLOT * (cw + 1) - 256 + lane * 16 : L_ZERO + min(lane, 11) * 16)) = u32x4{1u, 1u, 1u, 1u} * (cw < 3 ? 0x3f803f80u : 0u);
  SBAR();
  bf16x8 zz = {}; asm volatile("s_nop 1" : "+v"(zz));
  sfor<Cfg<FFN>::NRB * 4>([&](auto i) INL { mfma_new(acc[i / 4][i % 4], zz, zz); });
  SBAR();
  wait_vm<0>();
  u32x4 v[2];
  { const u32 a = L_ROWS + cw * 2048 + (lane >> 5) * XROWB + min(lane & 31, 27) * 32; v[0] = *(const LAS u32x4*)a; v[1] = *(const LAS u32x4*)(a + 16); }
  const u64 tag = uni64(*(const LAS u64*)(L_ROWS + 8192 + cw * 256)) + 1;
  f32x2 e[8]; f32x2 s2 = {0.f, 0.f};
  for (int c = 0; c < 8; c++) { e[c] = f32x2{bflo(v[c >> 2][c & 3]), bfhi(v[c >> 2][c & 3])}; s2 += e[c]; }
  const float mean = half_sum(act ? s2[0] + s2[1] : 0.f) * (1.0f / D_);
  f32x2 q2 = {0.f, 0.f};
  for (int c = 0; c < 8; c++) { const f32x2 d = e[c] - mean; q2 += d * d; }
  const float var = rbf(half_sum(act ? q2[0] + q2[1] : 0.f) * (1.0f / D_));
  const u32 res = pk(-mean, __builtin_amdgcn_sqrtf(rbf(var + 1e-5f)));
  if ((lane & 31) == 0) st64_sc1(ws + WS_STAT + srow * 8, (tag << 32) | res);
  if (cw == 0 && lane == 0) *(LAS u64*)L_TAG = tag;
  return tag;
}

__global__ void __launch_bounds__(512, 2) fused_kernel(Args A) {
  __shared__ __attribute__((aligned(16))) char smem[LDS_BYTES];
  asm volatile("" ::"s"((u32)(size_t)(LAS char*)smem));
  const int bid = blockIdx.x, lane = threadIdx.x & 63;
  const int wid = __builtin_amdgcn_readfirstlane(threadIdx.x >> 6);
  const u32 xcd = bid & 7, r = (bid >> 3) & 31, rg = xcd >> 1, chf = xcd & 1;
  u64 pc; asm volatile("s_getpc_b64 %0" : "=s"(pc));
  {
    const u32 o0 = min((r & 7) * 8 + wid, (u32)CODE_KB - 1) * 1024 + lane * 16;
    asm volatile("s_mov_b32 m0, %2\n\ts_nop 0\n\tglobal_load_lds_dwordx4 %0, %1" :: "v"(o0), "s"(pc & ~255ull), "i"(L_JUNK) : "memory");
  }
  if (wid < 4) {
    asm volatile("s_mov_b32 m0, %4\n\ts_nop 0\n\tglobal_load_lds_dwordx4 %0, %1\n\tglobal_load_lds_dwordx4 %6, %1 offset:1024\n\t"
                 "s_mov_b32 m0, %5\n\ts_nop 0\n\tglobal_load_lds_dword %2, %3 sc1"
                 :: "v"(lane * 16), "s"((const char*)A.x + (bid * 8 + wid * 2) * XROWB), "v"((lane & 1) * 4), "s"(A.ws + WS_EPOCH),
                    "s"(L_ROWS + wid * 2048), "s"(L_ROWS + 8192 + wid * 256), "v"(min(lane, 47) * 16) : "memory");
  }
  char* ws = A.ws;
  const bool is_qkv = r < 9;
  const int hw = wid - 4, cw = wid;
  const u32 coff = min((r & 15) * 4 + hw, (u32)CODE_KB - 1) * 1024;
  if (__builtin_expect(!is_qkv, 1)) {
    const u32 fi = rg * 23 + r - 9;
    const bool idle = fi >= 91;
    const u32 pr = (fi * 79) >> 10, po = fi - 13 * pr, up = (po + 10 - chf) >> 4;
    const int tile = po + 6 * chf - 6 * up, grp = 2 * pr + up;
    const u32 j2 = xcd * 23 - ((xcd + 1) >> 3) + r - 9;
    const int rt = (j2 * 147) >> 10, ct = j2 - 7 * rt;
    if (__builtin_expect(wid >= 4, 1)) {
      if (__builtin_expect(idle, 0)) { __builtin_amdgcn_endpgm(); }
      bf16x8 wdf[14][2];
      Side<true> sd;
      loader<true>([&](auto k) INL { if constexpr (k == 1) sd.prepare(A, hw, opq(lane), rt, ct); else sd.request(A); }, A, wdf, hw, lane, tile, grp, ct, bid, j2, coff, pc & ~255ull);
      const u64 tag = uni64(*(volatile LAS u64*)L_TAG);
      stage2<true>(A, sd, wdf, hw, lane, rt, ct, tag);
      return;
    }
    f32x4 acc[10][4];
    const u64 tag = compute_head<true>(A, acc, bid, cw, lane);
    if (idle) {
      if (xcd == 7 && cw == 0) {
#pragma unroll 1
        for (int i = 0; i < 16; i++) {
          const char* p = ws + WS_STAT + (i * 64 + lane) * 16;
#pragma unroll 1
          for (u32 sp = 0; sp < SPIN_CAP; sp++) { if (__all((u32)(ld64_sc1(p) >> 32) == (u32)tag)) break; __builtin_amdgcn_s_sleep(8); }
        }
        if (lane == 0) st64_sc1(ws + WS_EPOCH, tag);
      }
      __builtin_amdgcn_endpgm();
    }
    {
      const int g = lane >> 4, q = (lane >> 2) & 3, p = lane & 3;
      const u32 wrow = (8 * g + q) * 256, wsw = (q | ((g & 1) << 2)) << 2;
      u32 wa[4];
      for (int f = 0; f < 2; f++) wa[f] = wrow + (((8 * cw + 4 * f + p) ^ wsw) << 3);
      wa[2] = wa[3] = 0;
      const __amdgpu_buffer_rsrc_t ra = rsrc(ws + WS_ACT + (size_t)(2 * tile) * ACT_TILE, 2 * ACT_TILE);
      if (cw == 2) touch_at(ws + WS_HFLAG, (tile * 14 + grp) * FSTR);
      const u32 hb = opq((grp * 4 + cw) * 5120 + lane * 16);
      gemm1<true>(acc, wa, lane, [&](auto rc, u32x4 o) INL { constexpr int R = rc;
        __builtin_amdgcn_raw_buffer_store_b128(o, ra, hb, (R / 5) * ACT_TILE + (R % 5) * 1024, 16); });
      Side<false> sd;
      sd.prepare(A, cw, lane, rt, ct); sd.request(A);
      bf16x8 wdf[14][2];
      {
        u32 da[3];
        da[0] = opq(w2_lane_addr(lane, cw, 0)); da[1] = opq(da[0] + 61440); da[2] = opq(da[0] + 122880);
        bar();
        sfor<28>([&](auto u) INL { constexpr int o = cu_lds(u), hi = o / 61440, oo = o - hi * 61440;
          wdf[u >> 1][u & 1] = ldtr2(da[hi] + oo, da[hi] + oo + 2048); SBAR(); });
      }
      wait_lgkm0();
      bar();
      wait_vm<0>();
      if (lane == 0) st64_sc1(ws + WS_HFLAG + (tile * 14 + grp) * FSTR + cw * 8, tag);
      stage2<false>(A, sd, wdf, cw, lane, rt, ct, tag);
    }
    __builtin_amdgcn_endpgm();
  }
  const u32 qi = rg * 9 + r, grp = (qi * 43) >> 8;
  const int tile = chf * 6 + qi - 6 * grp;
  const int h0 = (0xDA8630 >> (4 * grp)) & 15;
  const int hh0 = (0x18 >> grp) & 1;
  AttSetup AS;
  if (__builtin_expect(wid >= 4, 1)) {
    bf16x8 dummy[14][2];
    loader<false>([&](auto part) INL { att_setup<part>(AS, hw, tile, opq(lane)); }, A, dummy, hw, lane, tile, grp, 0, bid, r * 8 + xcd, coff, pc & ~255ull);
    if (lane < 48) { const u32 a = (hw * 48 + lane) * IMGROW + 56; for (int i = 0; i < 9; i++) *(LAS u32x2*)(a + i * 64) = u32x2{0u, 0u}; }
  } else {
    f32x4 acc[12][4];
    {
      const int lane = opq(lane_id());
      compute_head<false>(A, acc, bid, cw, lane);
      const int g = lane >> 4, q = (lane >> 2) & 3, p = lane & 3;
      const u32 wsw = (q | ((g & 1) << 2)) << 2;
      u32 wa[4];
      if (cw == 2) touch_at(ws + WS_OFLAG, (tile * 6 + grp) * FSTR);
      const int Q0 = 7 * h0 - 16 * grp;
      for (int f = 0; f < 4; f++) {
        const int id = min(16 * cw + 4 * f + p, 62), m = (id * 49) >> 10;
        wa[f] = g * 6144 + m * 2048 + q * 256 + (((Q0 + id - 21 * m) ^ wsw) << 3);
      }
      gemm1<false>(acc, wa, lane, 0);
    }
    bar();
    const u32 l2 = opq(lane_id());
    f32x4 st[3], cs[4];
    { const u32 sta = opq(L_STAT + (l2 & 15) * 48); for (int i = 0; i < 3; i++) st[i] = lds128f(sta + i * 16); }
    SBAR();
    u32 io[4];
    sfor<4>([&](auto f) INL { for (int e = 0; e < 4; e++) cs[f][e] = dppf<0x15f>(acc[11][f][e]);
      const u32 id = 16 * cw + 4 * f + (l2 >> 4); io[f] = opq((l2 & 15) * IMGROW + ((id * 37) >> 8) * 8 + id * 8); });
    SBAR();
    sfor<12>([&](auto rc) INL { constexpr int R = rc;
      const float m = st[R >> 2][R & 3];
      if constexpr (R == 6) { for (int f = 0; f < 4; f++) io[f] = opq(io[f] + 6 * 16 * IMGROW); }
      sfor<4>([&](auto f) INL {
        const f32x4 v = acc[R][f] + m * cs[f];
        *(LAS u32x2*)(io[f] + (R % 6) * 16 * IMGROW) = u32x2{pk(v[0], v[1]), pk(v[2], v[3])};
        SBAR();
        if constexpr (R == 5 && f == 1) { asm volatile("s_waitcnt lgkmcnt(2)" ::: "memory"); bar(); }
        if constexpr (R == 10 && f == 0) { const u32 a = opq(L_REC + l2 * 16), b = a + cw * 4096;
          AS.b0.a = *(const LAS u32x4*)b; AS.b0.rl = lds128f(b + 1024); AS.b0.rk = lds128f(b + 2048); AS.b0.den = *(const LAS u32x4*)(b + 3072); AS.bias = lds128f(a + 16384); }
      });
    });
  }
  attention(A, AS, wid, tile, h0, hh0, opq(lane_id()));
  wait_vm<0>();
  bar();
  if (wid == 0 && lane_id() == 0) st64_sc1(ws + WS_OFLAG + (tile * 6 + grp) * FSTR, *(volatile LAS u64*)L_TAG);
  __builtin_amdgcn_endpgm();
}

extern "C" void kernel_launch(void* const* d_in, const int* in_sizes, int n_in, void* d_out, int out_size, void* d_ws, size_t ws_size, hipStream_t stream) {
  Args A;
  A.x = (const u16*)d_in[0]; A.wqkv = (const u16*)d_in[1]; A.wo = (const u16*)d_in[2];
  A.wg = (const u16*)d_in[4]; A.wu = (const u16*)d_in[5]; A.wd = (const u16*)d_in[6];
  A.out = (u16*)d_out; A.ws = (char*)d_ws;
  fused_kernel<<<256, 512, 0, stream>>>(A);
}
```
